```python
import torch

def _softmax(x, axis=-1):
    return torch.softmax(x, dim=axis)
def _gelu(x):
    return torch.nn.functional.gelu(x, approximate="tanh")
def _silu(x):
    return torch.nn.functional.silu(x)
def _sigmoid(x):
    return torch.sigmoid(x)
def _layernorm(x, axis=-1, eps=1e-5):
    m = x.mean(dim=axis, keepdim=True)
    v = x.var(dim=axis, keepdim=True, unbiased=False)
    return (x - m) / torch.sqrt(v + eps)
def _scatter_add(tgt, idx, src, axis=0):
    return tgt.clone().index_add_(axis, idx, src)
def _segment_sum(src, seg, n):
    out = torch.zeros((n,) + src.shape[1:], dtype=src.dtype)
    return out.index_add_(0, seg, src)
def _pad(x, pw):
    flat = tuple(v for pair in reversed(pw) for v in pair)
    return torch.nn.functional.pad(x, flat)

kernel_name = 'recurrent_scan__sliding_block_cached__swiglu_ffn__decode'

def setup_inputs():

    _w = (lambda a: (lambda t: t.to(torch.bfloat16) if t.is_floating_point() else t)(torch.as_tensor(a)))
    def _A_setup():
        B, Sq, D = 1, 1, 576
        Hq, Hkv, Sk, Dh = 32, 16, 8192, 128
        assert Hq % Hkv == 0, (Hq, Hkv)
        return {
            "x": _w((lambda *s: torch.randn(*s, dtype=torch.bfloat16) * 0.05)(B, Sq, D)),
            "k_cache": _w((lambda *s: torch.randn(*s, dtype=torch.bfloat16) * 0.8)(B, Hkv, Sk, Dh)),
            "v_cache": _w((lambda *s: torch.randn(*s, dtype=torch.bfloat16) * 0.2)(B, Hkv, Sk, Dh)),
            "wq": _w((lambda *s: torch.randn(*s, dtype=torch.bfloat16) * 0.8)(D, Hq * Dh) * (D ** -0.5)),
            "wo": _w((lambda *s: torch.randn(*s, dtype=torch.bfloat16) * 0.2)(Hq * Dh, D) * ((Hq * Dh) ** -0.5)),
        }
    _A_in = _A_setup()

    def _B_setup():
        B, S, D, F = 1, 1, 576, 4608
        return {
            "x": _w((lambda *s: torch.randn(*s, dtype=torch.bfloat16) * 0.05)(B, S, D)),
            "w_gate": _w((lambda *s: torch.randn(*s, dtype=torch.bfloat16) * 0.1)(D, F)),
            "w_up": _w((lambda *s: torch.randn(*s, dtype=torch.bfloat16) * 0.1)(D, F)),
            "w_down": _w((lambda *s: torch.randn(*s, dtype=torch.bfloat16) * 0.1)(F, D)),
        }
    _B_in = _B_setup()

    return {
        "A_x": _A_in["x"],
        "A_k_cache": _A_in["k_cache"],
        "A_v_cache": _A_in["v_cache"],
        "A_wq": _A_in["wq"],
        "A_wo": _A_in["wo"],
        "B_x": _B_in["x"],
        "B_w_gate": _B_in["w_gate"],
        "B_w_up": _B_in["w_up"],
        "B_w_down": _B_in["w_down"],
    }

def reference(A_x, A_k_cache, A_v_cache, A_wq, A_wo, B_x, B_w_gate, B_w_up, B_w_down):
    def _A_ref(x, k_cache, v_cache, wq, wo):
        B, Sq, D = x.shape
        Hq, Dh = 32, 128
        Hkv, Sk = k_cache.shape[1], k_cache.shape[2]
        W = min(4096, max(256, Sk // 4))
        rep = Hq // Hkv
        q = (lambda x, *d: x.permute(*d))((x @ wq).reshape(B, Sq, Hq, Dh), 0, 2, 1, 3)
        k = (lambda x, n, axis: torch.repeat_interleave(x, n, dim=axis))(k_cache[:, :, Sk - W:, :], rep, axis=1)
        v = (lambda x, n, axis: torch.repeat_interleave(x, n, dim=axis))(v_cache[:, :, Sk - W:, :], rep, axis=1)
        scores = torch.einsum("bhqd,bhkd->bhqk", q, k) * (Dh ** -0.5)
        p = _softmax(scores, axis=-1)
        o = torch.einsum("bhqk,bhkd->bhqd", p, v)
        return (lambda x, *d: x.permute(*d))(o, 0, 2, 1, 3).reshape(B, Sq, Hq * Dh) @ wo

    def _B_ref(x, w_gate, w_up, w_down):
        g = _silu(x @ w_gate)
        u = x @ w_up
        return (g * u) @ w_down

    x = A_x
    xn = _layernorm(x, axis=-1)
    s = _A_ref(xn, A_k_cache, A_v_cache, A_wq, A_wo)
    g = _silu(_B_ref(xn, B_w_gate, B_w_up, B_w_down))
    return x + s * g
```

```cpp
#include <hip/hip_runtime.h>
#include <hip/hip_bf16.h>
#include <stdint.h>

typedef unsigned short u16;
typedef unsigned int u32;
typedef u32 v4u __attribute__((ext_vector_type(4)));

constexpr int D = 576, HQ = 32, HKV = 16, SK = 8192, WIN = 2048, DH = 128, FF = 4608;
constexpr int NCH = 16, CK = WIN / NCH;

__device__ __forceinline__ float bflo(u32 v) { return __uint_as_float(v << 16); }
__device__ __forceinline__ float bfhi(u32 v) { return __uint_as_float(v & 0xffff0000u); }
__device__ __forceinline__ u16 f2bf(float f) { u32 u = __float_as_uint(f); u += 0x7fffu + ((u >> 16) & 1u); return (u16)(u >> 16); }
__device__ __forceinline__ float rbf(float f) { return __uint_as_float(((u32)f2bf(f)) << 16); }

__device__ __forceinline__ void layernorm_to_lds(const u16* __restrict__ x, float* xn, float* red  ) {
    const int tid = threadIdx.x, nt = blockDim.x, lane = tid & 63, wave = tid >> 6, nw = nt >> 6;
    float s = 0.f;
    for (int i = tid; i < D; i += nt) s += bflo((u32)x[i]);
    for (int o = 32; o >= 1; o >>= 1) s += __shfl_xor(s, o);
    if (lane == 0) red[wave] = s;
    __syncthreads();
    float tot = 0.f; for (int w = 0; w < nw; ++w) tot += red[w];
    const float mean = tot * (1.0f / D);
    __syncthreads();
    float v = 0.f;
    for (int i = tid; i < D; i += nt) { float d = bflo((u32)x[i]) - mean; v += d * d; }
    for (int o = 32; o >= 1; o >>= 1) v += __shfl_xor(v, o);
    if (lane == 0) red[wave] = v;
    __syncthreads();
    float vt = 0.f; for (int w = 0; w < nw; ++w) vt += red[w];
    const float var = rbf(vt * (1.0f / D));
    const float mb = rbf(mean);
    const float den = rbf(sqrtf(rbf(var + 1e-5f)));
    for (int i = tid; i < D; i += nt) xn[i] = rbf(rbf(bflo((u32)x[i]) - mb) / den);
    __syncthreads();
}

__global__ void __launch_bounds__(256) k1(const u16* __restrict__ x, const u16* __restrict__ wq, const u16* __restrict__ wg,
                                          const u16* __restrict__ wu, float* __restrict__ q_out, float* __restrict__ h_out) {
    __shared__ float xn[D];
    __shared__ float red[8];
    __shared__ float part[4][2][32];
    layernorm_to_lds(x, xn, red);
    const int tid = threadIdx.x, lane = tid & 63, wave = tid >> 6;
    const int l4 = tid & 3, rs = tid >> 2;
    const int task = blockIdx.x;
    const bool isq = task < 128;
    const int col0 = (isq ? task : task - 128) * 32;
    const int ld = isq ? HQ * DH : FF;
    const u16* w0 = isq ? wq : wg;
    float a0[8], a1[8];
#pragma unroll
    for (int j = 0; j < 8; ++j) a0[j] = a1[j] = 0.f;
#pragma unroll
    for (int it = 0; it < 9; ++it) {
        const int row = it * 64 + rs;
        const float xv = xn[row];
        const v4u w = *(const v4u*)(w0 + (size_t)row * ld + col0 + l4 * 8);
        a0[0] += xv * bflo(w.x); a0[1] += xv * bfhi(w.x); a0[2] += xv * bflo(w.y); a0[3] += xv * bfhi(w.y);
        a0[4] += xv * bflo(w.z); a0[5] += xv * bfhi(w.z); a0[6] += xv * bflo(w.w); a0[7] += xv * bfhi(w.w);
        if (!isq) {
            const v4u u = *(const v4u*)(wu + (size_t)row * ld + col0 + l4 * 8);
            a1[0] += xv * bflo(u.x); a1[1] += xv * bfhi(u.x); a1[2] += xv * bflo(u.y); a1[3] += xv * bfhi(u.y);
            a1[4] += xv * bflo(u.z); a1[5] += xv * bfhi(u.z); a1[6] += xv * bflo(u.w); a1[7] += xv * bfhi(u.w);
        }
    }
#pragma unroll
    for (int j = 0; j < 8; ++j) {
        for (int o = 4; o <= 32; o <<= 1) { a0[j] += __shfl_xor(a0[j], o); a1[j] += __shfl_xor(a1[j], o); }
    }
    if (lane < 4) {
#pragma unroll
        for (int j = 0; j < 8; ++j) { part[wave][0][lane * 8 + j] = a0[j]; part[wave][1][lane * 8 + j] = a1[j]; }
    }
    __syncthreads();
    if (tid < 32) {
        float s0 = part[0][0][tid] + part[1][0][tid] + part[2][0][tid] + part[3][0][tid];
        float s1 = part[0][1][tid] + part[1][1][tid] + part[2][1][tid] + part[3][1][tid];
        if (isq) q_out[col0 + tid] = rbf(s0);
        else {
            const float g = rbf(s0), u = rbf(s1);
            const float sg = rbf(g / (1.0f + __expf(-g)));
            h_out[col0 + tid] = rbf(sg * u);
        }
    }
}

__global__ void __launch_bounds__(256) k2(const u16* __restrict__ kc, const u16* __restrict__ vc, const float* __restrict__ q,
                                          const float* __restrict__ h, const u16* __restrict__ wd,
                                          float* __restrict__ part_o, float* __restrict__ stats, float* __restrict__ part_f) {
    const int tid = threadIdx.x, lane = tid & 63, wave = tid >> 6;
    if (blockIdx.x >= 256) {
        const int w = blockIdx.x - 256;
        for (int j8 = tid; j8 < D / 8; j8 += 256) {
            float a[8];
#pragma unroll
            for (int j = 0; j < 8; ++j) a[j] = 0.f;
            for (int r = 0; r < 18; ++r) {
                const float hv = h[18 * w + r];
                const v4u ww = *(const v4u*)(wd + (size_t)(18 * w + r) * D + j8 * 8);
                a[0] += hv * bflo(ww.x); a[1] += hv * bfhi(ww.x); a[2] += hv * bflo(ww.y); a[3] += hv * bfhi(ww.y);
                a[4] += hv * bflo(ww.z); a[5] += hv * bfhi(ww.z); a[6] += hv * bflo(ww.w); a[7] += hv * bfhi(ww.w);
            }
#pragma unroll
            for (int j = 0; j < 8; ++j) part_f[(size_t)w * D + j8 * 8 + j] = a[j];
        }
        return;
    }
    __shared__ float qs[2][DH];
    __shared__ float sc[2][CK];
    __shared__ float red[4][2][DH];
    __shared__ float mx[2], sm[2];
    const int g = blockIdx.x >> 4, c = blockIdx.x & 15;
    if (tid < 256) qs[tid >> 7][tid & 127] = q[g * 256 + tid];
    __syncthreads();
    const int l16 = tid & 15, ks = tid >> 4;
    const size_t base = ((size_t)g * SK + (SK - WIN) + (size_t)c * CK) * DH;
    float q0[8], q1[8];
#pragma unroll
    for (int j = 0; j < 8; ++j) { q0[j] = qs[0][l16 * 8 + j]; q1[j] = qs[1][l16 * 8 + j]; }
    const float scale = 0.08838834764831845f;
    for (int it = 0; it < CK / 16; ++it) {
        const int key = it * 16 + ks;
        const v4u kk = *(const v4u*)(kc + base + (size_t)key * DH + l16 * 8);
        float d0 = q0[0] * bflo(kk.x) + q0[1] * bfhi(kk.x) + q0[2] * bflo(kk.y) + q0[3] * bfhi(kk.y) + q0[4] * bflo(kk.z) + q0[5] * bfhi(kk.z) + q0[6] * bflo(kk.w) + q0[7] * bfhi(kk.w);
        float d1 = q1[0] * bflo(kk.x) + q1[1] * bfhi(kk.x) + q1[2] * bflo(kk.y) + q1[3] * bfhi(kk.y) + q1[4] * bflo(kk.z) + q1[5] * bfhi(kk.z) + q1[6] * bflo(kk.w) + q1[7] * bfhi(kk.w);
        for (int o = 1; o <= 8; o <<= 1) { d0 += __shfl_xor(d0, o); d1 += __shfl_xor(d1, o); }
        if (l16 == 0) { sc[0][key] = rbf(rbf(d0) * scale); sc[1][key] = rbf(rbf(d1) * scale); }
    }
    __syncthreads();
    if (wave < 2) {
        float a = sc[wave][lane], b = sc[wave][lane + 64];
        float m = fmaxf(a, b);
        for (int o = 32; o >= 1; o >>= 1) m = fmaxf(m, __shfl_xor(m, o));
        a = __expf(a - m); b = __expf(b - m);
        float s = a + b;
        for (int o = 32; o >= 1; o >>= 1) s += __shfl_xor(s, o);
        sc[wave][lane] = a; sc[wave][lane + 64] = b;
        if (lane == 0) { mx[wave] = m; sm[wave] = s; }
    }
    __syncthreads();
    float o0[8], o1[8];
#pragma unroll
    for (int j = 0; j < 8; ++j) o0[j] = o1[j] = 0.f;
    for (int it = 0; it < CK / 16; ++it) {
        const int key = it * 16 + ks;
        const v4u vv = *(const v4u*)(vc + base + (size_t)key * DH + l16 * 8);
        const float p0 = sc[0][key], p1 = sc[1][key];
        o0[0] += p0 * bflo(vv.x); o0[1] += p0 * bfhi(vv.x); o0[2] += p0 * bflo(vv.y); o0[3] += p0 * bfhi(vv.y);
        o0[4] += p0 * bflo(vv.z); o0[5] += p0 * bfhi(vv.z); o0[6] += p0 * bflo(vv.w); o0[7] += p0 * bfhi(vv.w);
        o1[0] += p1 * bflo(vv.x); o1[1] += p1 * bfhi(vv.x); o1[2] += p1 * bflo(vv.y); o1[3] += p1 * bfhi(vv.y);
        o1[4] += p1 * bflo(vv.z); o1[5] += p1 * bfhi(vv.z); o1[6] += p1 * bflo(vv.w); o1[7] += p1 * bfhi(vv.w);
    }
#pragma unroll
    for (int j = 0; j < 8; ++j) {
        o0[j] += __shfl_xor(o0[j], 16); o0[j] += __shfl_xor(o0[j], 32);
        o1[j] += __shfl_xor(o1[j], 16); o1[j] += __shfl_xor(o1[j], 32);
    }
    if (lane < 16) {
#pragma unroll
        for (int j = 0; j < 8; ++j) { red[wave][0][lane * 8 + j] = o0[j]; red[wave][1][lane * 8 + j] = o1[j]; }
    }
    __syncthreads();
    {
        const int hh = tid >> 7, d = tid & 127;
        const float v = red[0][hh][d] + red[1][hh][d] + red[2][hh][d] + red[3][hh][d];
        part_o[(((size_t)g * 2 + hh) * NCH + c) * DH + d] = v;
        if (d == 0) { stats[(((size_t)g * 2 + hh) * NCH + c) * 2 + 0] = mx[hh]; stats[(((size_t)g * 2 + hh) * NCH + c) * 2 + 1] = sm[hh]; }
    }
}

__global__ void __launch_bounds__(256) k3(const float* __restrict__ part_o, const float* __restrict__ stats,
                                          const u16* __restrict__ wo, float* __restrict__ part_s) {
    __shared__ float ov[16];
    const int tid = threadIdx.x, w = blockIdx.x;
    const int hq = w >> 3, d0 = (w & 7) * 16;
    if (tid < 16) {
        float M = -INFINITY;
        for (int c = 0; c < NCH; ++c) M = fmaxf(M, stats[((size_t)hq * NCH + c) * 2]);
        float L = 0.f, acc = 0.f;
        for (int c = 0; c < NCH; ++c) {
            const float e = __expf(stats[((size_t)hq * NCH + c) * 2] - M);
            L += e * stats[((size_t)hq * NCH + c) * 2 + 1];
            acc += e * part_o[((size_t)hq * NCH + c) * DH + d0 + tid];
        }
        ov[tid] = rbf(acc / L);
    }
    __syncthreads();
    for (int j8 = tid; j8 < D / 8; j8 += 256) {
        float a[8];
#pragma unroll
        for (int j = 0; j < 8; ++j) a[j] = 0.f;
        for (int r = 0; r < 16; ++r) {
            const float hv = ov[r];
            const v4u ww = *(const v4u*)(wo + (size_t)(16 * w + r) * D + j8 * 8);
            a[0] += hv * bflo(ww.x); a[1] += hv * bfhi(ww.x); a[2] += hv * bflo(ww.y); a[3] += hv * bfhi(ww.y);
            a[4] += hv * bflo(ww.z); a[5] += hv * bfhi(ww.z); a[6] += hv * bflo(ww.w); a[7] += hv * bfhi(ww.w);
        }
#pragma unroll
        for (int j = 0; j < 8; ++j) part_s[(size_t)w * D + j8 * 8 + j] = a[j];
    }
}

__global__ void __launch_bounds__(64) k4(const float* __restrict__ part_s, const float* __restrict__ part_f,
                                         const u16* __restrict__ x, u16* __restrict__ out) {
    const int j = blockIdx.x * 64 + threadIdx.x;
    float s = 0.f, f = 0.f;
    for (int w = 0; w < 256; ++w) { s += part_s[(size_t)w * D + j]; f += part_f[(size_t)w * D + j]; }
    s = rbf(s); f = rbf(f);
    const float g = rbf(f / (1.0f + __expf(-f)));
    out[j] = f2bf(bflo((u32)x[j]) + rbf(s * g));
}

extern "C" void kernel_launch(void* const* d_in, const int* in_sizes, int n_in,
                              void* d_out, int out_size, void* d_ws, size_t ws_size,
                              hipStream_t stream) {
    const u16* x = (const u16*)d_in[0];
    const u16* kc = (const u16*)d_in[1];
    const u16* vc = (const u16*)d_in[2];
    const u16* wq = (const u16*)d_in[3];
    const u16* wo = (const u16*)d_in[4];
    const u16* wg = (const u16*)d_in[6];
    const u16* wu = (const u16*)d_in[7];
    const u16* wd = (const u16*)d_in[8];
    float* ws = (float*)d_ws;
    float* q = ws;
    float* h = q + 4096;
    float* part_o = h + 4608;
    float* stats = part_o + 32 * 16 * 128;
    float* part_f = stats + 1024;
    float* part_s = part_f + 256 * 576;
    k1<<<272, 256, 0, stream>>>(x, wq, wg, wu, q, h);
    k2<<<512, 256, 0, stream>>>(kc, vc, q, h, wd, part_o, stats, part_f);
    k3<<<256, 256, 0, stream>>>(part_o, stats, wo, part_s);
    k4<<<9, 64, 0, stream>>>(part_s, part_f, x, (u16*)d_out);
}
```

```cpp
#include <hip/hip_runtime.h>
#include <hip/hip_bf16.h>
#include <stdint.h>

typedef unsigned short u16;
typedef unsigned int u32;
typedef unsigned long long u64;
typedef u32 v4u __attribute__((ext_vector_type(4)));
typedef u32 v3u __attribute__((ext_vector_type(3)));
typedef float v2f __attribute__((ext_vector_type(2)));
typedef float v4f __attribute__((ext_vector_type(4)));
typedef __bf16 bf16x2 __attribute__((ext_vector_type(2)));
#define GAS __attribute__((address_space(1)))

constexpr int D = 576, HQD = 4096, SK = 8192, WIN = 2048, DH = 128, FF = 4608;
constexpr int NWG = 256, NT = 1024;
constexpr int NRED = 144;
constexpr u32 WS_EPOCH = 0;
constexpr u32 WS_QG = 4096;
constexpr u32 WS_OG = WS_QG + 16 * 2048;
constexpr u32 WS_FG = WS_OG + 16 * 16 * 2304;
constexpr u32 WS_END = WS_FG + NRED * 16384;
constexpr u32 SPIN_CAP = 1u << 18;

#define STAMP(k) do { } while (0)

__device__ __forceinline__ float bflo(u32 v) { return __uint_as_float(v << 16); }
__device__ __forceinline__ float bfhi(u32 v) { return __uint_as_float(v & 0xffff0000u); }
__device__ __forceinline__ u32 f2bf(float f) { u32 u = __float_as_uint(f); u += 0x7fffu + ((u >> 16) & 1u); return u >> 16; }
__device__ __forceinline__ float rbf(float f) { return __uint_as_float(f2bf(f) << 16); }
__device__ __forceinline__ float dot2bf(u32 a, u32 b, float c) {
    return __builtin_amdgcn_fdot2_f32_bf16(__builtin_bit_cast(bf16x2, a), __builtin_bit_cast(bf16x2, b), c, false);
}
template <int CTRL> __device__ __forceinline__ float dpp(float v) {
    return __builtin_bit_cast(float, __builtin_amdgcn_update_dpp(0, __builtin_bit_cast(int, v), CTRL, 0xf, 0xf, true));
}
__device__ __forceinline__ float row16_sum(float v) {
    v += dpp<0xB1>(v); v += dpp<0x4E>(v); v += dpp<0x124>(v); v += dpp<0x128>(v); return v;
}
__device__ __forceinline__ float row16_max(float v) {
    v = fmaxf(v, dpp<0xB1>(v)); v = fmaxf(v, dpp<0x4E>(v)); v = fmaxf(v, dpp<0x124>(v)); v = fmaxf(v, dpp<0x128>(v)); return v;
}
__device__ __forceinline__ void xrow_pair32(float v, float& a, float& b) {
    a = v; b = v;
    asm volatile("s_nop 1\n\tv_permlane32_swap_b32 %0, %1\n\ts_nop 1" : "+v"(a), "+v"(b));
}
__device__ __forceinline__ void xrow_pair16(float v, float& a, float& b) {
    a = v; b = v;
    asm volatile("s_nop 1\n\tv_permlane16_swap_b32 %0, %1\n\ts_nop 1" : "+v"(a), "+v"(b));
}
__device__ __forceinline__ float xrow_sum(float v) {
    float a, b; xrow_pair32(v, a, b); v = a + b; xrow_pair16(v, a, b); return a + b;
}
__device__ __forceinline__ float xrow_max(float v) {
    float a, b; xrow_pair32(v, a, b); v = fmaxf(a, b); xrow_pair16(v, a, b); return fmaxf(a, b);
}
__device__ __forceinline__ v4u ldg16(const u16* p) { return *(const v4u*)p; }
__device__ __forceinline__ v3u ldg12(const u16* p) { return *(const v3u*)p; }
__device__ __forceinline__ v4u poll_until(__amdgpu_buffer_rsrc_t rs, int off, v4u v, u32 tagA, u32 tagB, bool active) {
    bool ok = !active || ((v.x == tagA) & (v.z == tagB));
    if (!__all(ok)) {
        for (u32 spins = 0; spins < SPIN_CAP; ++spins) {
            __builtin_amdgcn_s_sleep(1);
            int o2 = off;
            asm volatile("" : "+v"(o2) :: "memory");
            const v4u nv = __builtin_amdgcn_raw_buffer_load_b128(rs, o2, 0, 16);
            if (!ok) v = nv;
            ok = !active || ((v.x == tagA) & (v.z == tagB));
            if (__all(ok)) break;
        }
    }
    return v;
}
#define SCHED_FENCE() __builtin_amdgcn_sched_barrier(0)
#define TOUCH(v) asm volatile("" :: "v"(v))

__global__ void __launch_bounds__(NT, 1)
fused_k(const u16* __restrict__ x, const u16* __restrict__ kc, const u16* __restrict__ vc, const u16* __restrict__ wq,
        const u16* __restrict__ wo, const u16* __restrict__ wg, const u16* __restrict__ wu, const u16* __restrict__ wd,
        u16* __restrict__ out, unsigned char* __restrict__ ws) {
    __shared__ __attribute__((aligned(16))) float xn_l[D];
    __shared__ __attribute__((aligned(16))) float qred[32][16];
    __shared__ __attribute__((aligned(16))) u32 q16[128];
    __shared__ __attribute__((aligned(16))) float sc[2][128];
    __shared__ __attribute__((aligned(16))) float pvred[16][256];
    __shared__ __attribute__((aligned(16))) float f1red[36][288];
    __shared__ __attribute__((aligned(16))) float gu[64];
    __shared__ __attribute__((aligned(16))) float f2red[9][D];
    __shared__ __attribute__((aligned(16))) float o2red[8][D];
    __shared__ __attribute__((aligned(16))) float og[16][20];
    __shared__ __attribute__((aligned(16))) float fin[16][8];

    const int t = threadIdx.x, lane = t & 63;
    const int wave = __builtin_amdgcn_readfirstlane(t >> 6);
    const int b = blockIdx.x;
    const int w = (b & 7) * 32 + (b >> 3);
    const int g = w >> 4, j = w & 15;
    const __amdgpu_buffer_rsrc_t rs = __builtin_amdgcn_make_buffer_rsrc((void*)ws, 0, (int)WS_END, 0x00020000);
    STAMP(0);

    u32 epoch = __hip_atomic_load((GAS u32*)(ws + WS_EPOCH), __ATOMIC_RELAXED, __HIP_MEMORY_SCOPE_AGENT);
    const v4u xp0 = ldg16(x + lane * 8);
    const v4u xp1r = ldg16(x + 512 + (lane & 7) * 8);
    const bool isq = wave < 8, isq3 = wave < 2;
    const int hf = t & 1, rsq = (t >> 1) & 255;
    const u32 xr0 = x[isq ? rsq : 0], xr1 = x[isq ? rsq + 256 : 0], xr2 = x[isq3 ? rsq + 512 : 0];
    const u16* wp = wq + (size_t)rsq * HQD + w * 16 + hf * 8;
    const v4u wq0 = ldg16(isq ? wp : wq);
    const v4u wq1 = ldg16(isq ? wp + (size_t)256 * HQD : wq);
    const v4u wq2 = ldg16(isq3 ? wp + (size_t)512 * HQD : wq);
    const int part = t & 15, ks = t >> 4;
    const size_t kvoff = ((size_t)g * SK + (SK - WIN) + (size_t)j * 128) * DH + part * 8;
    const v4u k0 = ldg16(kc + kvoff + (size_t)ks * DH);
    SCHED_FENCE();
    STAMP(1);

    const v4u xp1 = (lane < 8) ? xp1r : (v4u){0u, 0u, 0u, 0u};
    float xsum = (bflo(xp0.x) + bfhi(xp0.x)) + (bflo(xp0.y) + bfhi(xp0.y)) + (bflo(xp0.z) + bfhi(xp0.z)) + (bflo(xp0.w) + bfhi(xp0.w))
               + (bflo(xp1.x) + bfhi(xp1.x)) + (bflo(xp1.y) + bfhi(xp1.y)) + (bflo(xp1.z) + bfhi(xp1.z)) + (bflo(xp1.w) + bfhi(xp1.w));
    xsum = xrow_sum(row16_sum(xsum));
    const float mean = xsum * (1.0f / D);
    float vs;
    {
        float d, a = 0.f;
#define SQ(u) d = bflo(u) - mean; a += d * d; d = bfhi(u) - mean; a += d * d;
        SQ(xp0.x) SQ(xp0.y) SQ(xp0.z) SQ(xp0.w)
        if (lane < 8) { SQ(xp1.x) SQ(xp1.y) SQ(xp1.z) SQ(xp1.w) }
#undef SQ
        vs = xrow_sum(row16_sum(a));
    }
    const float mb = rbf(mean);
    const float rden = rbf(sqrtf(rbf(rbf(vs * (1.0f / D)) + 1e-5f)));
    STAMP(2);
    if (isq) {
        const float xn0 = rbf(rbf(bflo(xr0) - mb) / rden), xn1 = rbf(rbf(bflo(xr1) - mb) / rden);
        const float xn2 = isq3 ? rbf(rbf(bflo(xr2) - mb) / rden) : 0.f;
        if (hf == 0) { xn_l[rsq] = xn0; xn_l[rsq + 256] = xn1; if (isq3) xn_l[rsq + 512] = xn2; }
        float a[8];
#define QF(i, fn, c) a[i] = xn0 * fn(wq0.c) + xn1 * fn(wq1.c) + xn2 * fn(wq2.c);
        QF(0, bflo, x) QF(1, bfhi, x) QF(2, bflo, y) QF(3, bfhi, y) QF(4, bflo, z) QF(5, bfhi, z) QF(6, bflo, w) QF(7, bfhi, w)
#undef QF
#pragma unroll
        for (int i = 0; i < 8; ++i) { a[i] += dpp<0x4E>(a[i]); a[i] += dpp<0x124>(a[i]); a[i] += dpp<0x128>(a[i]); }
        if ((lane & 15) < 2) {
            float* qp = &qred[wave * 4 + (lane >> 4)][hf * 8];
            *(v4f*)qp = (v4f){a[0], a[1], a[2], a[3]};
            *(v4f*)(qp + 4) = (v4f){a[4], a[5], a[6], a[7]};
        }
    }
    STAMP(3);
    const v4u k1 = ldg16(kc + kvoff + (size_t)(ks + 64) * DH);
    SCHED_FENCE();
    __syncthreads();
    epoch = __builtin_amdgcn_readfirstlane(epoch);
    const u32 tagA = epoch + 1u, tagB = (epoch + 1u) * 0x9E3779B1u ^ 0x7F4A7C15u;
    if (wave == 0) {
        const int cp = lane & 7, p8 = lane >> 3;
        v2f s = *(const v2f*)&qred[p8 * 4 + 0][2 * cp];
        v2f s1 = *(const v2f*)&qred[p8 * 4 + 1][2 * cp];
        v2f s2 = *(const v2f*)&qred[p8 * 4 + 2][2 * cp];
        v2f s3 = *(const v2f*)&qred[p8 * 4 + 3][2 * cp];
        float sx = (s.x + s1.x) + (s2.x + s3.x), sy = (s.y + s1.y) + (s2.y + s3.y);
        sx += dpp<0x128>(sx); sy += dpp<0x128>(sy);
        sx = xrow_sum(sx); sy = xrow_sum(sy);
        if (lane < 8) {
            const v4u dg = {tagA, f2bf(sx) << 16, tagB, f2bf(sy) << 16};
            __builtin_amdgcn_raw_buffer_store_b128(dg, rs, (int)(WS_QG + g * 2048 + (j * 8 + cp) * 16), 0, 16);
        }
    }
    STAMP(4);
    const int qoff = (int)(WS_QG + g * 2048 + (wave * 64 + lane) * 16);
    v4u pq = {0u, 0u, 0u, 0u};
    if (wave < 2) pq = __builtin_amdgcn_raw_buffer_load_b128(rs, qoff, 0, 16);
    SCHED_FENCE();
    TOUCH(k0);
    const v4u v0 = ldg16(vc + kvoff + (size_t)ks * DH);
    SCHED_FENCE();
    TOUCH(k1);
    const v4u v1 = ldg16(vc + kvoff + (size_t)(ks + 64) * DH);
    SCHED_FENCE();
    if (wave < 2) {
        pq = poll_until(rs, qoff, pq, tagA, tagB, true);
        q16[wave * 64 + lane] = (pq.y >> 16) | (pq.w & 0xffff0000u);
    }
    __syncthreads();
    STAMP(5);
    {
        const v4u qa = *(const v4u*)&q16[part * 4], qb = *(const v4u*)&q16[64 + part * 4];
        float d00 = dot2bf(qa.x, k0.x, 0.f), d01 = dot2bf(qa.x, k1.x, 0.f), d10 = dot2bf(qb.x, k0.x, 0.f), d11 = dot2bf(qb.x, k1.x, 0.f);
        d00 = dot2bf(qa.y, k0.y, d00); d01 = dot2bf(qa.y, k1.y, d01); d10 = dot2bf(qb.y, k0.y, d10); d11 = dot2bf(qb.y, k1.y, d11);
        d00 = dot2bf(qa.z, k0.z, d00); d01 = dot2bf(qa.z, k1.z, d01); d10 = dot2bf(qb.z, k0.z, d10); d11 = dot2bf(qb.z, k1.z, d11);
        d00 = dot2bf(qa.w, k0.w, d00); d01 = dot2bf(qa.w, k1.w, d01); d10 = dot2bf(qb.w, k0.w, d10); d11 = dot2bf(qb.w, k1.w, d11);
        d00 = row16_sum(d00); d01 = row16_sum(d01); d10 = row16_sum(d10); d11 = row16_sum(d11);
        if (part == 0) {
            const float scale = 0.08838834764831845f;
            sc[0][ks] = rbf(rbf(d00) * scale); sc[0][ks + 64] = rbf(rbf(d01) * scale);
            sc[1][ks] = rbf(rbf(d10) * scale); sc[1][ks + 64] = rbf(rbf(d11) * scale);
        }
    }
    __syncthreads();
    STAMP(6);
    float m0, m1, l0, l1, p00, p01, p10, p11;
    {
        const float a0 = sc[0][lane], b0 = sc[0][lane + 64], a1 = sc[1][lane], b1 = sc[1][lane + 64];
        m0 = xrow_max(row16_max(fmaxf(a0, b0))); m1 = xrow_max(row16_max(fmaxf(a1, b1)));
        l0 = xrow_sum(row16_sum(__expf(a0 - m0) + __expf(b0 - m0)));
        l1 = xrow_sum(row16_sum(__expf(a1 - m1) + __expf(b1 - m1)));
        p00 = __expf(sc[0][ks] - m0); p01 = __expf(sc[0][ks + 64] - m0);
        p10 = __expf(sc[1][ks] - m1); p11 = __expf(sc[1][ks + 64] - m1);
    }
    const bool isf1 = t < 864, wf1 = wave < 14;
    const int t3 = isf1 ? t : 863;
    const int cp3 = t3 % 3, rs3 = t3 / 3;
    const size_t goff = (size_t)rs3 * FF + w * 18 + cp3 * 6;
    STAMP(7);
    TOUCH(v0);
    const v3u g0 = ldg12(wf1 ? wg + goff : wg), g1 = ldg12(wf1 ? wg + goff + (size_t)288 * FF : wg);
    SCHED_FENCE();
    TOUCH(v1);
    const v3u u0 = ldg12(wf1 ? wu + goff : wu), u1 = ldg12(wf1 ? wu + goff + (size_t)288 * FF : wu);
    SCHED_FENCE();
    STAMP(8);
    {
        float o0[8], o1[8];
#define PV(i, fn, c) { const float a = fn(v0.c), bb = fn(v1.c); o0[i] = p00 * a + p01 * bb; o1[i] = p10 * a + p11 * bb; }
        PV(0, bflo, x) PV(1, bfhi, x) PV(2, bflo, y) PV(3, bfhi, y) PV(4, bflo, z) PV(5, bfhi, z) PV(6, bflo, w) PV(7, bfhi, w)
#undef PV
#pragma unroll
        for (int i = 0; i < 8; ++i) { o0[i] = xrow_sum(o0[i]); o1[i] = xrow_sum(o1[i]); }
        if (lane < 16) {
            float* pp = &pvred[wave][part * 8];
            *(v4f*)pp = (v4f){o0[0], o0[1], o0[2], o0[3]};
            *(v4f*)(pp + 4) = (v4f){o0[4], o0[5], o0[6], o0[7]};
            *(v4f*)(pp + 128) = (v4f){o1[0], o1[1], o1[2], o1[3]};
            *(v4f*)(pp + 132) = (v4f){o1[4], o1[5], o1[6], o1[7]};
        }
    }
    __syncthreads();
    STAMP(9);
    if (t < 128) {
        float sx = 0.f, sy = 0.f;
#pragma unroll
        for (int ww = 0; ww < 16; ++ww) { const v2f pv = *(const v2f*)&pvred[ww][2 * t]; sx += pv.x; sy += pv.y; }
        const int d = (2 * t) & 127, h = t >> 6;
        const int cons = h * 8 + (d >> 4), e = (d & 15) >> 1;
        const v4u dg = {tagA, __float_as_uint(sx), tagB, __float_as_uint(sy)};
        __builtin_amdgcn_raw_buffer_store_b128(dg, rs, (int)(WS_OG + ((g * 16 + cons) * 16 + j) * 144 + e * 16), 0, 16);
    } else if (t < 144) {
        const int cons = t - 128, h = cons >> 3;
        const v4u dg = {tagA, __float_as_uint(h ? m1 : m0), tagB, __float_as_uint(h ? l1 : l0)};
        __builtin_amdgcn_raw_buffer_store_b128(dg, rs, (int)(WS_OG + ((g * 16 + cons) * 16 + j) * 144 + 128), 0, 16);
    }
    STAMP(10);
    const int oidx = wave * 64 + lane;
    const bool isop = (wave < 3) && (oidx < 144);
    const int ooff = (int)(WS_OG + (g * 16 + j) * 2304 + oidx * 16);
    v4u po = {0u, 0u, 0u, 0u};
    if (wave < 3) po = __builtin_amdgcn_raw_buffer_load_b128(rs, ooff, 0, 16);
    SCHED_FENCE();
    const bool isd = t < 648, wdn = wave < 11;
    const int td = isd ? t : 647;
    const int cpc = td % 72, rsl = td / 72;
    TOUCH(g0); TOUCH(g1);
    const v4u dn0 = ldg16(wdn ? wd + (size_t)(w * 18 + rsl) * D + cpc * 8 : wd);
    SCHED_FENCE();
    TOUCH(u0); TOUCH(u1);
    const v4u dn1 = ldg16(wdn ? wd + (size_t)(w * 18 + rsl + 9) * D + cpc * 8 : wd);
    SCHED_FENCE();
    STAMP(11);
    if (isf1) {
        const float xa = xn_l[rs3], xb = xn_l[rs3 + 288];
#define F1(m, i, fn, c, A, B) f1red[(m) * 18 + cp3 * 6 + (i)][rs3] = xa * fn(A.c) + xb * fn(B.c);
        F1(0, 0, bflo, x, g0, g1) F1(0, 1, bfhi, x, g0, g1) F1(0, 2, bflo, y, g0, g1) F1(0, 3, bfhi, y, g0, g1) F1(0, 4, bflo, z, g0, g1) F1(0, 5, bfhi, z, g0, g1)
        F1(1, 0, bflo, x, u0, u1) F1(1, 1, bfhi, x, u0, u1) F1(1, 2, bflo, y, u0, u1) F1(1, 3, bfhi, y, u0, u1) F1(1, 4, bflo, z, u0, u1) F1(1, 5, bfhi, z, u0, u1)
#undef F1
    }
    __syncthreads();
    if (t < 576) {
        const int oi = t >> 4, sub = t & 15;
        float a = 0.f;
#pragma unroll
        for (int i = 0; i < 18; ++i) a += f1red[oi][sub + 16 * i];
        a = row16_sum(a);
        if (sub == 0) gu[oi] = a;
    }
    __syncthreads();
    STAMP(12);
    const bool iso = wave < 9;
    TOUCH(dn0);
    const v4u ow0 = ldg16(iso ? wo + (size_t)(w * 16 + rsl) * D + cpc * 8 : wo);
    SCHED_FENCE();
    TOUCH(dn1);
    const v4u ow1 = ldg16(iso ? wo + (size_t)(w * 16 + rsl + 8) * D + cpc * 8 : wo);
    SCHED_FENCE();
    STAMP(13);
    if (isd) {
        float h0, h1;
        {
            const float ga = rbf(gu[rsl]), ua = rbf(gu[18 + rsl]), gb = rbf(gu[rsl + 9]), ub = rbf(gu[18 + rsl + 9]);
            h0 = rbf(rbf(ga / (1.0f + __expf(-ga))) * ua);
            h1 = rbf(rbf(gb / (1.0f + __expf(-gb))) * ub);
        }
        float* fp = &f2red[rsl][cpc * 8];
        *(v4f*)fp = (v4f){h0 * bflo(dn0.x) + h1 * bflo(dn1.x), h0 * bfhi(dn0.x) + h1 * bfhi(dn1.x), h0 * bflo(dn0.y) + h1 * bflo(dn1.y), h0 * bfhi(dn0.y) + h1 * bfhi(dn1.y)};
        *(v4f*)(fp + 4) = (v4f){h0 * bflo(dn0.z) + h1 * bflo(dn1.z), h0 * bfhi(dn0.z) + h1 * bfhi(dn1.z), h0 * bflo(dn0.w) + h1 * bflo(dn1.w), h0 * bfhi(dn0.w) + h1 * bfhi(dn1.w)};
    }
    if (wave < 3) {
        po = poll_until(rs, ooff, po, tagA, tagB, isop);
        if (isop) { const int c = oidx / 9, e = oidx % 9; *(v2f*)&og[c][2 * e] = (v2f){__uint_as_float(po.y), __uint_as_float(po.w)}; }
    }
    __syncthreads();
    STAMP(14);
    const int fr = t >> 2, k4 = t & 3;
    if (t < 576 && k4 >= 2) {
        float sx = 0.f, sy = 0.f;
#pragma unroll
        for (int i = 0; i < 9; ++i) { const v2f pv = *(const v2f*)&f2red[i][4 * fr + 2 * (k4 - 2)]; sx += pv.x; sy += pv.y; }
        const v4u dg = {tagA, __float_as_uint(sx), tagB, __float_as_uint(sy)};
        __builtin_amdgcn_raw_buffer_store_b128(dg, rs, (int)(WS_FG + ((fr * 256 + w) * 4 + k4) * 16), 0, 16);
    }
    if (iso) {
        float M = og[0][16];
#pragma unroll
        for (int c = 1; c < 16; ++c) M = fmaxf(M, og[c][16]);
        float L = 0.f, oa = 0.f, ob = 0.f;
#pragma unroll
        for (int c = 0; c < 16; ++c) {
            const float e = __expf(og[c][16] - M);
            L += e * og[c][17]; oa += e * og[c][rsl]; ob += e * og[c][rsl + 8];
        }
        const float rl = 1.0f / L;
        oa = rbf(oa * rl); ob = rbf(ob * rl);
        float* op = &o2red[rsl][cpc * 8];
        *(v4f*)op = (v4f){oa * bflo(ow0.x) + ob * bflo(ow1.x), oa * bfhi(ow0.x) + ob * bfhi(ow1.x), oa * bflo(ow0.y) + ob * bflo(ow1.y), oa * bfhi(ow0.y) + ob * bfhi(ow1.y)};
        *(v4f*)(op + 4) = (v4f){oa * bflo(ow0.z) + ob * bflo(ow1.z), oa * bfhi(ow0.z) + ob * bfhi(ow1.z), oa * bflo(ow0.w) + ob * bflo(ow1.w), oa * bfhi(ow0.w) + ob * bfhi(ow1.w)};
    }
    __syncthreads();
    if (t < 576 && k4 < 2) {
        float sx = 0.f, sy = 0.f;
#pragma unroll
        for (int i = 0; i < 8; ++i) { const v2f pv = *(const v2f*)&o2red[i][4 * fr + 2 * k4]; sx += pv.x; sy += pv.y; }
        const v4u dg = {tagA, __float_as_uint(sx), tagB, __float_as_uint(sy)};
        __builtin_amdgcn_raw_buffer_store_b128(dg, rs, (int)(WS_FG + ((fr * 256 + w) * 4 + k4) * 16), 0, 16);
    }
    STAMP(15);
    if (j >= 9) return;
    const int r = g * 9 + j;
    {
        const int foff = (int)(WS_FG + r * 16384 + t * 16);
        v4u pf = __builtin_amdgcn_raw_buffer_load_b128(rs, foff, 0, 16);
        pf = poll_until(rs, foff, pf, tagA, tagB, true);
        STAMP(16);
        float sx = __uint_as_float(pf.y), sy = __uint_as_float(pf.w);
        sx += dpp<0x124>(sx); sx += dpp<0x128>(sx); sy += dpp<0x124>(sy); sy += dpp<0x128>(sy);
        sx = xrow_sum(sx); sy = xrow_sum(sy);
        if (lane < 4) *(v2f*)&fin[wave][lane * 2] = (v2f){sx, sy};
    }
    __syncthreads();
    if (t < 4) {
        float s = 0.f, f = 0.f;
#pragma unroll
        for (int ww = 0; ww < 16; ++ww) { s += fin[ww][t]; f += fin[ww][4 + t]; }
        s = rbf(s); f = rbf(f);
        const float gt = rbf(f / (1.0f + __expf(-f)));
        const int col = 4 * r + t;
        out[col] = (u16)f2bf(bflo((u32)x[col]) + rbf(s * gt));
        if (r == 0 && t == 0) __hip_atomic_store((GAS u32*)(ws + WS_EPOCH), tagA, __ATOMIC_RELAXED, __HIP_MEMORY_SCOPE_AGENT);
    }
    STAMP(17);
}

extern "C" void kernel_launch(void* const* d_in, const int* in_sizes, int n_in,
                              void* d_out, int out_size, void* d_ws, size_t ws_size,
                              hipStream_t stream) {
    fused_k<<<NWG, NT, 0, stream>>>((const u16*)d_in[0], (const u16*)d_in[1], (const u16*)d_in[2], (const u16*)d_in[3], (const u16*)d_in[4],
                                    (const u16*)d_in[6], (const u16*)d_in[7], (const u16*)d_in[8], (u16*)d_out, (unsigned char*)d_ws);
}
```

```cpp
#include <hip/hip_runtime.h>
#include <hip/hip_bf16.h>
#include <stdint.h>

typedef unsigned short u16;
typedef unsigned int u32;
typedef unsigned long long u64;
typedef u32 v4u __attribute__((ext_vector_type(4)));
typedef u32 v3u __attribute__((ext_vector_type(3)));
typedef float v2f __attribute__((ext_vector_type(2)));
typedef float v4f __attribute__((ext_vector_type(4)));
typedef __bf16 bf16x2 __attribute__((ext_vector_type(2)));
#define GAS __attribute__((address_space(1)))

constexpr int D = 576, HQD = 4096, SK = 8192, WIN = 2048, DH = 128, FF = 4608;
constexpr int NWG = 256, NT = 1024;
constexpr int NRED = 72;
constexpr u32 WS_EPOCH = 0;
constexpr u32 WS_QG = 4096;
constexpr u32 WS_OG = WS_QG + 16 * 2048;
constexpr u32 WS_FG = WS_OG + 16 * 8 * 8 * 272;
constexpr u32 WS_END = WS_FG + NRED * 16384;
constexpr u32 SPIN_CAP = 1u << 18;

#define STAMP(k) do { } while (0)

__device__ __forceinline__ float bflo(u32 v) { return __uint_as_float(v << 16); }
__device__ __forceinline__ float bfhi(u32 v) { return __uint_as_float(v & 0xffff0000u); }
__device__ __forceinline__ u32 f2bf(float f) { u32 u = __float_as_uint(f); u += 0x7fffu + ((u >> 16) & 1u); return u >> 16; }
__device__ __forceinline__ float rbf(float f) { return __uint_as_float(f2bf(f) << 16); }
__device__ __forceinline__ float dot2bf(u32 a, u32 b, float c) {
    return __builtin_amdgcn_fdot2_f32_bf16(__builtin_bit_cast(bf16x2, a), __builtin_bit_cast(bf16x2, b), c, false);
}
template <int CTRL> __device__ __forceinline__ float dpp(float v) {
    return __builtin_bit_cast(float, __builtin_amdgcn_update_dpp(0, __builtin_bit_cast(int, v), CTRL, 0xf, 0xf, true));
}
__device__ __forceinline__ float row16_sum(float v) {
    v += dpp<0xB1>(v); v += dpp<0x4E>(v); v += dpp<0x124>(v); v += dpp<0x128>(v); return v;
}
__device__ __forceinline__ float row16_max(float v) {
    v = fmaxf(v, dpp<0xB1>(v)); v = fmaxf(v, dpp<0x4E>(v)); v = fmaxf(v, dpp<0x124>(v)); v = fmaxf(v, dpp<0x128>(v)); return v;
}
__device__ __forceinline__ void xrow_pair32(float v, float& a, float& b) {
    a = v; b = v;
    asm volatile("s_nop 1\n\tv_permlane32_swap_b32 %0, %1\n\ts_nop 1" : "+v"(a), "+v"(b));
}
__device__ __forceinline__ void xrow_pair16(float v, float& a, float& b) {
    a = v; b = v;
    asm volatile("s_nop 1\n\tv_permlane16_swap_b32 %0, %1\n\ts_nop 1" : "+v"(a), "+v"(b));
}
__device__ __forceinline__ float xrow_sum(float v) {
    float a, b; xrow_pair32(v, a, b); v = a + b; xrow_pair16(v, a, b); return a + b;
}
__device__ __forceinline__ float xrow_max(float v) {
    float a, b; xrow_pair32(v, a, b); v = fmaxf(a, b); xrow_pair16(v, a, b); return fmaxf(a, b);
}
__device__ __forceinline__ v4u ldg16(const u16* p) { return *(const v4u*)p; }
__device__ __forceinline__ v3u ldg12(const u16* p) { return *(const v3u*)p; }
__device__ __forceinline__ v4u poll_until(__amdgpu_buffer_rsrc_t rs, int off, u32 tagA, u32 tagB, bool active) {
    v4u v = {0u, 0u, 0u, 0u};
    bool ok = !active;
    for (u32 spins = 0; spins < SPIN_CAP; ++spins) {
        int o2 = off;
        asm volatile("" : "+v"(o2) :: "memory");
        const v4u nv = __builtin_amdgcn_raw_buffer_load_b128(rs, o2, 0, 16);
        if (!ok) v = nv;
        ok = !active || ((v.x == tagA) & (v.z == tagB));
        if (__all(ok)) break;
        __builtin_amdgcn_s_sleep(1);
    }
    return v;
}
#define SCHED_FENCE() __builtin_amdgcn_sched_barrier(0)
#define TOUCH(v) asm volatile("" :: "v"(v))

__global__ void __launch_bounds__(NT, 1)
fused_k(const u16* __restrict__ x, const u16* __restrict__ kc, const u16* __restrict__ vc, const u16* __restrict__ wq,
        const u16* __restrict__ wo, const u16* __restrict__ wg, const u16* __restrict__ wu, const u16* __restrict__ wd,
        u16* __restrict__ out, unsigned char* __restrict__ ws) {
    __shared__ __attribute__((aligned(16))) float pool[24576];
    const int t = threadIdx.x, lane = t & 63;
    const int wave = __builtin_amdgcn_readfirstlane(t >> 6);
    const int b = blockIdx.x;
    const int idx = b >> 3;
    const int li = (b & 7) * 16 + (idx >> 1);
    const bool is_attn = (idx & 1) == 0;
    const __amdgpu_buffer_rsrc_t rs = __builtin_amdgcn_make_buffer_rsrc((void*)ws, 0, (int)WS_END, 0x00020000);
    STAMP(0);
    const u64 tstart = __builtin_amdgcn_s_memrealtime();
    u32 epoch = __hip_atomic_load((GAS u32*)(ws + WS_EPOCH), __ATOMIC_RELAXED, __HIP_MEMORY_SCOPE_AGENT);

    if (!is_attn) {
        float* xn_l = pool;
        float* qred = pool + 576;
        float* f1red = pool + 1600;
        float* gu = pool + 14080;
        float* f2red = pool + 14336;
        u32* qcnt = (u32*)(pool + 1100);
        if (t == 0) *qcnt = 0u;
        __syncthreads();
        const int t6 = t < 1020 ? t : 1019;
        const int cp = t6 % 6, rs6 = t6 / 6;
        const bool r4 = rs6 < 66;
        const size_t goff = (size_t)rs6 * FF + li * 36 + cp * 6;
        const size_t goff3 = (size_t)(r4 ? rs6 + 510 : rs6) * FF + li * 36 + cp * 6;
        const int td = t < 864 ? t : 863;
        const int cpc = td % 72, rsl = td / 72;
        const u16* dp = wd + (size_t)(li * 36 + rsl) * D + cpc * 8;
        v3u g0, g1, g2, g3, u0, u1, u2, u3; v4u dn0, dn1, dn2;
        epoch = __builtin_amdgcn_readfirstlane(epoch);
        const u32 tagA = epoch + 1u, tagB = (epoch + 1u) * 0x9E3779B1u ^ 0x7F4A7C15u;
        if (wave < 4) {
            const v4u xp0 = ldg16(x + lane * 8);
            const v4u xp1r = ldg16(x + 512 + (lane & 7) * 8);
            const int q4 = t & 3, rq = t >> 2;
            u32 xr[9]; v4u wv[9];
#pragma unroll
            for (int i = 0; i < 9; ++i) xr[i] = x[rq + 64 * i];
#pragma unroll
            for (int i = 0; i < 9; ++i) wv[i] = ldg16(wq + (size_t)(rq + 64 * i) * HQD + li * 32 + q4 * 8);
            SCHED_FENCE();
            STAMP(1);
            const v4u xp1 = (lane < 8) ? xp1r : (v4u){0u, 0u, 0u, 0u};
            float xsum = (bflo(xp0.x) + bfhi(xp0.x)) + (bflo(xp0.y) + bfhi(xp0.y)) + (bflo(xp0.z) + bfhi(xp0.z)) + (bflo(xp0.w) + bfhi(xp0.w))
                       + (bflo(xp1.x) + bfhi(xp1.x)) + (bflo(xp1.y) + bfhi(xp1.y)) + (bflo(xp1.z) + bfhi(xp1.z)) + (bflo(xp1.w) + bfhi(xp1.w));
            xsum = xrow_sum(row16_sum(xsum));
            const float mean = xsum * (1.0f / D);
            float vs;
            {
                float d, a = 0.f;
#define SQ(u) d = bflo(u) - mean; a += d * d; d = bfhi(u) - mean; a += d * d;
                SQ(xp0.x) SQ(xp0.y) SQ(xp0.z) SQ(xp0.w)
                if (lane < 8) { SQ(xp1.x) SQ(xp1.y) SQ(xp1.z) SQ(xp1.w) }
#undef SQ
                vs = xrow_sum(row16_sum(a));
            }
            const float mb = rbf(mean);
            const float rden = rbf(sqrtf(rbf(rbf(vs * (1.0f / D)) + 1e-5f)));
            STAMP(2);
            float a[8] = {0.f, 0.f, 0.f, 0.f, 0.f, 0.f, 0.f, 0.f};
#pragma unroll
            for (int i = 0; i < 9; ++i) {
                const float xn = rbf(rbf(bflo(xr[i]) - mb) / rden);
                if (q4 == 0) xn_l[rq + 64 * i] = xn;
                a[0] += xn * bflo(wv[i].x); a[1] += xn * bfhi(wv[i].x); a[2] += xn * bflo(wv[i].y); a[3] += xn * bfhi(wv[i].y);
                a[4] += xn * bflo(wv[i].z); a[5] += xn * bfhi(wv[i].z); a[6] += xn * bflo(wv[i].w); a[7] += xn * bfhi(wv[i].w);
            }
#pragma unroll
            for (int i = 0; i < 8; ++i) { a[i] += dpp<0x124>(a[i]); a[i] += dpp<0x128>(a[i]); a[i] = xrow_sum(a[i]); }
            if (lane < 4) {
                float* qp = &qred[wave * 32 + q4 * 8];
                *(v4f*)qp = (v4f){a[0], a[1], a[2], a[3]};
                *(v4f*)(qp + 4) = (v4f){a[4], a[5], a[6], a[7]};
            }
            __builtin_amdgcn_fence(__ATOMIC_RELEASE, "workgroup");
            u32 arr = 0;
            if (lane == 0) arr = __hip_atomic_fetch_add(qcnt, 1u, __ATOMIC_RELAXED, __HIP_MEMORY_SCOPE_WORKGROUP);
            arr = __builtin_amdgcn_readfirstlane(arr);
            if (arr == 3u) {
                __builtin_amdgcn_fence(__ATOMIC_ACQUIRE, "workgroup");
                if (lane < 16) {
                    float sx = 0.f, sy = 0.f;
#pragma unroll
                    for (int ww = 0; ww < 4; ++ww) { const v2f p = *(const v2f*)&qred[ww * 32 + 2 * lane]; sx += p.x; sy += p.y; }
                    const v4u dg = {tagA, f2bf(sx) << 16, tagB, f2bf(sy) << 16};
                    __builtin_amdgcn_raw_buffer_store_b128(dg, rs, (int)(WS_QG + (li >> 3) * 2048 + ((li & 7) * 16 + lane) * 16), 0, 16);
                }
            }
            STAMP(4);
        }
        g0 = ldg12(wg + goff); g1 = ldg12(wg + goff + (size_t)170 * FF); g2 = ldg12(wg + goff + (size_t)340 * FF); g3 = ldg12(wg + goff3);
        u0 = ldg12(wu + goff); u1 = ldg12(wu + goff + (size_t)170 * FF); u2 = ldg12(wu + goff + (size_t)340 * FF); u3 = ldg12(wu + goff3);
        dn0 = ldg16(dp); dn1 = ldg16(dp + (size_t)12 * D); dn2 = ldg16(dp + (size_t)24 * D);
        SCHED_FENCE();
        STAMP(5);
        __syncthreads();
        if (t < 1020) {
            const float xa = xn_l[rs6], xb = xn_l[rs6 + 170], xc = xn_l[rs6 + 340], xd = r4 ? xn_l[rs6 + 510] : 0.f;
#define F1(m, i, fn, c, A, B, C, E) f1red[((m) * 36 + cp * 6 + (i)) * 170 + rs6] = (xa * fn(A.c) + xb * fn(B.c)) + (xc * fn(C.c) + xd * fn(E.c));
            F1(0, 0, bflo, x, g0, g1, g2, g3) F1(0, 1, bfhi, x, g0, g1, g2, g3) F1(0, 2, bflo, y, g0, g1, g2, g3)
            F1(0, 3, bfhi, y, g0, g1, g2, g3) F1(0, 4, bflo, z, g0, g1, g2, g3) F1(0, 5, bfhi, z, g0, g1, g2, g3)
            F1(1, 0, bflo, x, u0, u1, u2, u3) F1(1, 1, bfhi, x, u0, u1, u2, u3) F1(1, 2, bflo, y, u0, u1, u2, u3)
            F1(1, 3, bfhi, y, u0, u1, u2, u3) F1(1, 4, bflo, z, u0, u1, u2, u3) F1(1, 5, bfhi, z, u0, u1, u2, u3)
#undef F1
        }
        __syncthreads();
        STAMP(6);
        if (t < 576) {
            const int oi = t >> 3, sub = t & 7;
            float a = 0.f;
            for (int i = sub; i < 170; i += 8) a += f1red[oi * 170 + i];
            a += dpp<0xB1>(a); a += dpp<0x4E>(a); a += dpp<0x141>(a);
            if (sub == 0) gu[oi] = a;
        }
        __syncthreads();
        if (t < 864) {
            float h[3];
#pragma unroll
            for (int i = 0; i < 3; ++i) {
                const float ga = rbf(gu[rsl + 12 * i]), ua = rbf(gu[36 + rsl + 12 * i]);
                h[i] = rbf(rbf(ga / (1.0f + __expf(-ga))) * ua);
            }
            float* fp = &f2red[rsl * D + cpc * 8];
#define F2(c, fn) (h[0] * fn(dn0.c) + h[1] * fn(dn1.c) + h[2] * fn(dn2.c))
            *(v4f*)fp = (v4f){F2(x, bflo), F2(x, bfhi), F2(y, bflo), F2(y, bfhi)};
            *(v4f*)(fp + 4) = (v4f){F2(z, bflo), F2(z, bfhi), F2(w, bflo), F2(w, bfhi)};
#undef F2
        }
        __syncthreads();
        STAMP(7);
        if (t < 288) {
            float sx = 0.f, sy = 0.f;
#pragma unroll
            for (int i = 0; i < 12; ++i) { const v2f p = *(const v2f*)&f2red[i * D + 2 * t]; sx += p.x; sy += p.y; }
            const v4u dg = {tagA, __float_as_uint(sx), tagB, __float_as_uint(sy)};
            __builtin_amdgcn_raw_buffer_store_b128(dg, rs, (int)(WS_FG + (t >> 2) * 16384 + 8192 + (li * 4 + (t & 3)) * 16), 0, 16);
        }
        STAMP(8);
        return;
    }

    float* sc = pool;
    u32* q16 = (u32*)(pool + 512);
    float* pvred = pool + 1024;
    float* o2red = pool + 5120;
    float* og = pool + 9728;
    float* fin = pool + 10240;
    const int g = li >> 3, c = li & 7;
    const int part = t & 15, ks = t >> 4;
    const size_t kvoff = ((size_t)g * SK + (SK - WIN) + (size_t)c * 256) * DH + part * 8;
    const v4u k0 = ldg16(kc + kvoff + (size_t)ks * DH), k1 = ldg16(kc + kvoff + (size_t)(ks + 64) * DH);
    const v4u k2 = ldg16(kc + kvoff + (size_t)(ks + 128) * DH), k3 = ldg16(kc + kvoff + (size_t)(ks + 192) * DH);
    const v4u v0 = ldg16(vc + kvoff + (size_t)ks * DH), v1 = ldg16(vc + kvoff + (size_t)(ks + 64) * DH);
    const v4u v2 = ldg16(vc + kvoff + (size_t)(ks + 128) * DH), v3 = ldg16(vc + kvoff + (size_t)(ks + 192) * DH);
    const bool iso = wave < 9;
    const int cpc = t % 72, rsl = t / 72;
    const u16* op = wo + (size_t)(li * 32 + rsl) * D + cpc * 8;
    const v4u ow0 = ldg16(iso ? op : wo), ow1 = ldg16(iso ? op + (size_t)8 * D : wo), ow2 = ldg16(iso ? op + (size_t)16 * D : wo), ow3 = ldg16(iso ? op + (size_t)24 * D : wo);
    SCHED_FENCE();

    STAMP(1);
    epoch = __builtin_amdgcn_readfirstlane(epoch);
    const u32 tagA = epoch + 1u, tagB = (epoch + 1u) * 0x9E3779B1u ^ 0x7F4A7C15u;
    if (wave < 2) {
        const v4u pq = poll_until(rs, (int)(WS_QG + g * 2048 + (wave * 64 + lane) * 16), tagA, tagB, true);
        q16[wave * 64 + lane] = (pq.y >> 16) | (pq.w & 0xffff0000u);
    }
    __syncthreads();
    STAMP(2);
    {
        const v4u qa = *(const v4u*)&q16[part * 4], qb = *(const v4u*)&q16[64 + part * 4];
        float da[4], db[4];
#define SCORE(i, K) { float s0 = dot2bf(qa.x, K.x, 0.f), s1 = dot2bf(qb.x, K.x, 0.f); s0 = dot2bf(qa.y, K.y, s0); s1 = dot2bf(qb.y, K.y, s1); \
                      s0 = dot2bf(qa.z, K.z, s0); s1 = dot2bf(qb.z, K.z, s1); s0 = dot2bf(qa.w, K.w, s0); s1 = dot2bf(qb.w, K.w, s1); \
                      da[i] = row16_sum(s0); db[i] = row16_sum(s1); }
        SCORE(0, k0) SCORE(1, k1) SCORE(2, k2) SCORE(3, k3)
#undef SCORE
        if (part == 0) {
            const float scale = 0.08838834764831845f;
#pragma unroll
            for (int i = 0; i < 4; ++i) { sc[ks + 64 * i] = rbf(rbf(da[i]) * scale); sc[256 + ks + 64 * i] = rbf(rbf(db[i]) * scale); }
        }
    }
    __syncthreads();
    STAMP(3);
    float m0, m1, l0, l1, pa[4], pb[4];
    {
        const v4f a = *(const v4f*)&sc[lane * 4], bq = *(const v4f*)&sc[256 + lane * 4];
        m0 = xrow_max(row16_max(fmaxf(fmaxf(a.x, a.y), fmaxf(a.z, a.w))));
        m1 = xrow_max(row16_max(fmaxf(fmaxf(bq.x, bq.y), fmaxf(bq.z, bq.w))));
        l0 = xrow_sum(row16_sum((__expf(a.x - m0) + __expf(a.y - m0)) + (__expf(a.z - m0) + __expf(a.w - m0))));
        l1 = xrow_sum(row16_sum((__expf(bq.x - m1) + __expf(bq.y - m1)) + (__expf(bq.z - m1) + __expf(bq.w - m1))));
#pragma unroll
        for (int i = 0; i < 4; ++i) { pa[i] = __expf(sc[ks + 64 * i] - m0); pb[i] = __expf(sc[256 + ks + 64 * i] - m1); }
    }
    STAMP(4);
    {
        float o0[8], o1[8];
#define PV(i, fn, cc) { const float a = fn(v0.cc), bb = fn(v1.cc), cx = fn(v2.cc), dx = fn(v3.cc); \
                        o0[i] = (pa[0] * a + pa[1] * bb) + (pa[2] * cx + pa[3] * dx); o1[i] = (pb[0] * a + pb[1] * bb) + (pb[2] * cx + pb[3] * dx); }
        PV(0, bflo, x) PV(1, bfhi, x) PV(2, bflo, y) PV(3, bfhi, y) PV(4, bflo, z) PV(5, bfhi, z) PV(6, bflo, w) PV(7, bfhi, w)
#undef PV
#pragma unroll
        for (int i = 0; i < 8; ++i) { o0[i] = xrow_sum(o0[i]); o1[i] = xrow_sum(o1[i]); }
        if (lane < 16) {
            float* pp = &pvred[wave * 256 + part * 8];
            *(v4f*)pp = (v4f){o0[0], o0[1], o0[2], o0[3]};
            *(v4f*)(pp + 4) = (v4f){o0[4], o0[5], o0[6], o0[7]};
            *(v4f*)(pp + 128) = (v4f){o1[0], o1[1], o1[2], o1[3]};
            *(v4f*)(pp + 132) = (v4f){o1[4], o1[5], o1[6], o1[7]};
        }
    }
    __syncthreads();
    STAMP(5);
    if (t < 128) {
        float sx = 0.f, sy = 0.f;
#pragma unroll
        for (int ww = 0; ww < 16; ++ww) { const v2f pv = *(const v2f*)&pvred[ww * 256 + 2 * t]; sx += pv.x; sy += pv.y; }
        const int d = (2 * t) & 127, h = t >> 6;
        const int cons = h * 4 + (d >> 5), e = (d & 31) >> 1;
        const v4u dg = {tagA, __float_as_uint(sx), tagB, __float_as_uint(sy)};
        __builtin_amdgcn_raw_buffer_store_b128(dg, rs, (int)(WS_OG + ((g * 8 + cons) * 8 + c) * 272 + e * 16), 0, 16);
    } else if (t < 136) {
        const int cons = t - 128, h = cons >> 2;
        const v4u dg = {tagA, __float_as_uint(h ? m1 : m0), tagB, __float_as_uint(h ? l1 : l0)};
        __builtin_amdgcn_raw_buffer_store_b128(dg, rs, (int)(WS_OG + ((g * 8 + cons) * 8 + c) * 272 + 256), 0, 16);
    }
    STAMP(6);
    if (wave < 3) {
        const int oidx = wave * 64 + lane;
        const bool act = oidx < 136;
        const v4u po = poll_until(rs, (int)(WS_OG + (g * 8 + c) * 2176 + oidx * 16), tagA, tagB, act);
        if (act) { const int pc = oidx / 17, e = oidx % 17; *(v2f*)&og[pc * 36 + 2 * e] = (v2f){__uint_as_float(po.y), __uint_as_float(po.w)}; }
    }
    __syncthreads();
    STAMP(7);
    if (iso) {
        float M = og[32];
#pragma unroll
        for (int pc = 1; pc < 8; ++pc) M = fmaxf(M, og[pc * 36 + 32]);
        float L = 0.f, oa[4] = {0.f, 0.f, 0.f, 0.f};
#pragma unroll
        for (int pc = 0; pc < 8; ++pc) {
            const float e = __expf(og[pc * 36 + 32] - M);
            L += e * og[pc * 36 + 33];
#pragma unroll
            for (int i = 0; i < 4; ++i) oa[i] += e * og[pc * 36 + rsl + 8 * i];
        }
        const float rl = 1.0f / L;
#pragma unroll
        for (int i = 0; i < 4; ++i) oa[i] = rbf(oa[i] * rl);
        float* opp = &o2red[rsl * D + cpc * 8];
#define OP(cc, fn) ((oa[0] * fn(ow0.cc) + oa[1] * fn(ow1.cc)) + (oa[2] * fn(ow2.cc) + oa[3] * fn(ow3.cc)))
        *(v4f*)opp = (v4f){OP(x, bflo), OP(x, bfhi), OP(y, bflo), OP(y, bfhi)};
        *(v4f*)(opp + 4) = (v4f){OP(z, bflo), OP(z, bfhi), OP(w, bflo), OP(w, bfhi)};
#undef OP
    }
    __syncthreads();
    if (t < 288) {
        float sx = 0.f, sy = 0.f;
#pragma unroll
        for (int i = 0; i < 8; ++i) { const v2f p = *(const v2f*)&o2red[i * D + 2 * t]; sx += p.x; sy += p.y; }
        const v4u dg = {tagA, __float_as_uint(sx), tagB, __float_as_uint(sy)};
        __builtin_amdgcn_raw_buffer_store_b128(dg, rs, (int)(WS_FG + (t >> 2) * 16384 + (li * 4 + (t & 3)) * 16), 0, 16);
    }
    STAMP(8);
    if ((li & 15) >= 9) return;
    const int r = (li >> 4) * 9 + (li & 15);
    {
        const v4u pf = poll_until(rs, (int)(WS_FG + r * 16384 + t * 16), tagA, tagB, true);
        STAMP(9);
        float sx = __uint_as_float(pf.y), sy = __uint_as_float(pf.w);
        sx += dpp<0x124>(sx); sx += dpp<0x128>(sx); sy += dpp<0x124>(sy); sy += dpp<0x128>(sy);
        sx = xrow_sum(sx); sy = xrow_sum(sy);
        if (lane < 4) *(v2f*)&fin[wave * 8 + lane * 2] = (v2f){sx, sy};
    }
    __syncthreads();
    if (t < 8) {
        float s = 0.f, f = 0.f;
#pragma unroll
        for (int ww = 0; ww < 8; ++ww) { s += fin[ww * 8 + t]; f += fin[(8 + ww) * 8 + t]; }
        s = rbf(s); f = rbf(f);
        const float gt = rbf(f / (1.0f + __expf(-f)));
        const int col = 8 * r + t;
        out[col] = (u16)f2bf(bflo((u32)x[col]) + rbf(s * gt));
        if (r == 0 && t == 0) __hip_atomic_store((GAS u32*)(ws + WS_EPOCH), tagA, __ATOMIC_RELAXED, __HIP_MEMORY_SCOPE_AGENT);
    }
    STAMP(10);
}

extern "C" void kernel_launch(void* const* d_in, const int* in_sizes, int n_in,
                              void* d_out, int out_size, void* d_ws, size_t ws_size,
                              hipStream_t stream) {
    fused_k<<<NWG, NT, 0, stream>>>((const u16*)d_in[0], (const u16*)d_in[1], (const u16*)d_in[2], (const u16*)d_in[3], (const u16*)d_in[4],
                                    (const u16*)d_in[6], (const u16*)d_in[7], (const u16*)d_in[8], (u16*)d_out, (unsigned char*)d_ws);
}
```

```cpp
#include <hip/hip_runtime.h>
#include <hip/hip_bf16.h>
#include <stdint.h>

typedef unsigned short u16;
typedef unsigned int u32;
typedef unsigned long long u64;
typedef u32 v4u __attribute__((ext_vector_type(4)));
typedef u32 v3u __attribute__((ext_vector_type(3)));
typedef float v2f __attribute__((ext_vector_type(2)));
typedef float v4f __attribute__((ext_vector_type(4)));
typedef __bf16 bf16x2 __attribute__((ext_vector_type(2)));
#define GAS __attribute__((address_space(1)))

constexpr int D = 576, HQD = 4096, SK = 8192, WIN = 2048, DH = 128, FF = 4608;
constexpr int NWG = 256, NT = 1024;
constexpr int NRED = 72;
constexpr u32 WS_EPOCH = 0;
constexpr u32 WS_QG = 4096;
constexpr u32 WS_OG = WS_QG + 16 * 2048;
constexpr u32 WS_FG = WS_OG + 16 * 8 * 8 * 272;
constexpr u32 WS_END = WS_FG + NRED * 16384;
constexpr u32 SPIN_CAP = 1u << 18;

#define STAMP(k) do { } while (0)

__device__ __forceinline__ float bflo(u32 v) { return __uint_as_float(v << 16); }
__device__ __forceinline__ float bfhi(u32 v) { return __uint_as_float(v & 0xffff0000u); }
__device__ __forceinline__ u32 f2bf(float f) { u32 u = __float_as_uint(f); u += 0x7fffu + ((u >> 16) & 1u); return u >> 16; }
__device__ __forceinline__ float rbf(float f) { return __uint_as_float(f2bf(f) << 16); }
__device__ __forceinline__ float dot2bf(u32 a, u32 b, float c) {
    return __builtin_amdgcn_fdot2_f32_bf16(__builtin_bit_cast(bf16x2, a), __builtin_bit_cast(bf16x2, b), c, false);
}
template <int CTRL> __device__ __forceinline__ float dpp(float v) {
    return __builtin_bit_cast(float, __builtin_amdgcn_update_dpp(0, __builtin_bit_cast(int, v), CTRL, 0xf, 0xf, true));
}
__device__ __forceinline__ float row16_sum(float v) {
    v += dpp<0xB1>(v); v += dpp<0x4E>(v); v += dpp<0x124>(v); v += dpp<0x128>(v); return v;
}
__device__ __forceinline__ float row16_max(float v) {
    v = fmaxf(v, dpp<0xB1>(v)); v = fmaxf(v, dpp<0x4E>(v)); v = fmaxf(v, dpp<0x124>(v)); v = fmaxf(v, dpp<0x128>(v)); return v;
}
__device__ __forceinline__ void xrow_pair32(float v, float& a, float& b) {
    a = v; b = v;
    asm volatile("s_nop 1\n\tv_permlane32_swap_b32 %0, %1\n\ts_nop 1" : "+v"(a), "+v"(b));
}
__device__ __forceinline__ void xrow_pair16(float v, float& a, float& b) {
    a = v; b = v;
    asm volatile("s_nop 1\n\tv_permlane16_swap_b32 %0, %1\n\ts_nop 1" : "+v"(a), "+v"(b));
}
__device__ __forceinline__ float xrow_sum(float v) {
    float a, b; xrow_pair32(v, a, b); v = a + b; xrow_pair16(v, a, b); return a + b;
}
__device__ __forceinline__ float xrow_max(float v) {
    float a, b; xrow_pair32(v, a, b); v = fmaxf(a, b); xrow_pair16(v, a, b); return fmaxf(a, b);
}
__device__ __forceinline__ v4u ldg16(const u16* p) { return *(const v4u*)p; }
__device__ __forceinline__ v3u ldg12(const u16* p) { return *(const v3u*)p; }
__device__ __forceinline__ v4u poll_until(__amdgpu_buffer_rsrc_t rs, int off, u32 tagA, u32 tagB, bool active) {
    v4u v = {0u, 0u, 0u, 0u};
    bool ok = !active;
    for (u32 spins = 0; spins < SPIN_CAP; ++spins) {
        int o2 = off;
        asm volatile("" : "+v"(o2) :: "memory");
        const v4u nv = __builtin_amdgcn_raw_buffer_load_b128(rs, o2, 0, 16);
        if (!ok) v = nv;
        ok = !active || ((v.x == tagA) & (v.z == tagB));
        if (__all(ok)) break;
        __builtin_amdgcn_s_sleep(1);
    }
    return v;
}
#define SCHED_FENCE() __builtin_amdgcn_sched_barrier(0)
#define TOUCH(v) asm volatile("" :: "v"(v))

__global__ void __launch_bounds__(NT, 1)
fused_k(const u16* __restrict__ x, const u16* __restrict__ kc, const u16* __restrict__ vc, const u16* __restrict__ wq,
        const u16* __restrict__ wo, const u16* __restrict__ wg, const u16* __restrict__ wu, const u16* __restrict__ wd,
        u16* __restrict__ out, unsigned char* __restrict__ ws) {
    __shared__ __attribute__((aligned(16))) float pool[24576];
    const int t = threadIdx.x, lane = t & 63;
    const int wave = __builtin_amdgcn_readfirstlane(t >> 6);
    const int b = blockIdx.x;
    const int idx = b >> 3;
    const int li = (b & 7) * 16 + (idx >> 1);
    const bool is_attn = (idx & 1) == 0;
    const __amdgpu_buffer_rsrc_t rs = __builtin_amdgcn_make_buffer_rsrc((void*)ws, 0, (int)WS_END, 0x00020000);
    STAMP(0);
    const u64 tstart = __builtin_amdgcn_s_memrealtime();
    u32 epoch = __hip_atomic_load((GAS u32*)(ws + WS_EPOCH), __ATOMIC_RELAXED, __HIP_MEMORY_SCOPE_AGENT);

    if (!is_attn) {
        float* xn_l = pool;
        float* qred = pool + 576;
        float* f1red = pool + 1600;
        float* gu = pool + 14080;
        float* f2red = pool + 14336;
        u32* qcnt = (u32*)(pool + 1100);
        if (t == 0) *qcnt = 0u;
        __syncthreads();
        const int t6 = t < 1020 ? t : 1019;
        const int cp = t6 % 6, rs6 = t6 / 6;
        const bool r4 = rs6 < 66;
        const size_t goff = (size_t)rs6 * FF + li * 36 + cp * 6;
        const size_t goff3 = (size_t)(r4 ? rs6 + 510 : rs6) * FF + li * 36 + cp * 6;
        const int td = t < 864 ? t : 863;
        const int cpc = td % 72, rsl = td / 72;
        const u16* dp = wd + (size_t)(li * 36 + rsl) * D + cpc * 8;
        v3u g0, g1, g2, g3, u0, u1, u2, u3; v4u dn0, dn1, dn2;
        epoch = __builtin_amdgcn_readfirstlane(epoch);
        const u32 tagA = epoch + 1u, tagB = (epoch + 1u) * 0x9E3779B1u ^ 0x7F4A7C15u;
        if (wave < 4) {
            const v4u xp0 = ldg16(x + lane * 8);
            const v4u xp1r = ldg16(x + 512 + (lane & 7) * 8);
            const int q4 = t & 3, rq = t >> 2;
            u32 xr[9]; v4u wv[9];
#pragma unroll
            for (int i = 0; i < 9; ++i) xr[i] = x[rq + 64 * i];
#pragma unroll
            for (int i = 0; i < 9; ++i) wv[i] = ldg16(wq + (size_t)(rq + 64 * i) * HQD + li * 32 + q4 * 8);
            SCHED_FENCE();
            STAMP(1);
            const v4u xp1 = (lane < 8) ? xp1r : (v4u){0u, 0u, 0u, 0u};
            float xsum = (bflo(xp0.x) + bfhi(xp0.x)) + (bflo(xp0.y) + bfhi(xp0.y)) + (bflo(xp0.z) + bfhi(xp0.z)) + (bflo(xp0.w) + bfhi(xp0.w))
                       + (bflo(xp1.x) + bfhi(xp1.x)) + (bflo(xp1.y) + bfhi(xp1.y)) + (bflo(xp1.z) + bfhi(xp1.z)) + (bflo(xp1.w) + bfhi(xp1.w));
            xsum = xrow_sum(row16_sum(xsum));
            const float mean = xsum * (1.0f / D);
            float vs;
            {
                float d, a = 0.f;
#define SQ(u) d = bflo(u) - mean; a += d * d; d = bfhi(u) - mean; a += d * d;
                SQ(xp0.x) SQ(xp0.y) SQ(xp0.z) SQ(xp0.w)
                if (lane < 8) { SQ(xp1.x) SQ(xp1.y) SQ(xp1.z) SQ(xp1.w) }
#undef SQ
                vs = xrow_sum(row16_sum(a));
            }
            const float mb = rbf(mean);
            const float rden = rbf(sqrtf(rbf(rbf(vs * (1.0f / D)) + 1e-5f)));
            STAMP(2);
            float a[8] = {0.f, 0.f, 0.f, 0.f, 0.f, 0.f, 0.f, 0.f};
#pragma unroll
            for (int i = 0; i < 9; ++i) {
                const float xn = rbf(rbf(bflo(xr[i]) - mb) / rden);
                if (q4 == 0) xn_l[rq + 64 * i] = xn;
                a[0] += xn * bflo(wv[i].x); a[1] += xn * bfhi(wv[i].x); a[2] += xn * bflo(wv[i].y); a[3] += xn * bfhi(wv[i].y);
                a[4] += xn * bflo(wv[i].z); a[5] += xn * bfhi(wv[i].z); a[6] += xn * bflo(wv[i].w); a[7] += xn * bfhi(wv[i].w);
            }
#pragma unroll
            for (int i = 0; i < 8; ++i) { a[i] += dpp<0x124>(a[i]); a[i] += dpp<0x128>(a[i]); a[i] = xrow_sum(a[i]); }
            if (lane < 4) {
                float* qp = &qred[wave * 32 + q4 * 8];
                *(v4f*)qp = (v4f){a[0], a[1], a[2], a[3]};
                *(v4f*)(qp + 4) = (v4f){a[4], a[5], a[6], a[7]};
            }
            __builtin_amdgcn_fence(__ATOMIC_RELEASE, "workgroup");
            u32 arr = 0;
            if (lane == 0) arr = __hip_atomic_fetch_add(qcnt, 1u, __ATOMIC_RELAXED, __HIP_MEMORY_SCOPE_WORKGROUP);
            arr = __builtin_amdgcn_readfirstlane(arr);
            if (arr == 3u) {
                __builtin_amdgcn_fence(__ATOMIC_ACQUIRE, "workgroup");
                if (lane < 16) {
                    float sx = 0.f, sy = 0.f;
#pragma unroll
                    for (int ww = 0; ww < 4; ++ww) { const v2f p = *(const v2f*)&qred[ww * 32 + 2 * lane]; sx += p.x; sy += p.y; }
                    const v4u dg = {tagA, f2bf(sx) << 16, tagB, f2bf(sy) << 16};
                    __builtin_amdgcn_raw_buffer_store_b128(dg, rs, (int)(WS_QG + (li >> 3) * 2048 + ((li & 7) * 16 + lane) * 16), 0, 16);
                }
            }
            STAMP(4);
        }
        g0 = ldg12(wg + goff); g1 = ldg12(wg + goff + (size_t)170 * FF); g2 = ldg12(wg + goff + (size_t)340 * FF); g3 = ldg12(wg + goff3);
        u0 = ldg12(wu + goff); u1 = ldg12(wu + goff + (size_t)170 * FF); u2 = ldg12(wu + goff + (size_t)340 * FF); u3 = ldg12(wu + goff3);
        dn0 = ldg16(dp); dn1 = ldg16(dp + (size_t)12 * D); dn2 = ldg16(dp + (size_t)24 * D);
        SCHED_FENCE();
        STAMP(5);
        __syncthreads();
        if (t < 1020) {
            const float xa = xn_l[rs6], xb = xn_l[rs6 + 170], xc = xn_l[rs6 + 340], xd = r4 ? xn_l[rs6 + 510] : 0.f;
#define F1(m, i, fn, c, A, B, C, E) f1red[((m) * 36 + cp * 6 + (i)) * 170 + rs6] = (xa * fn(A.c) + xb * fn(B.c)) + (xc * fn(C.c) + xd * fn(E.c));
            F1(0, 0, bflo, x, g0, g1, g2, g3) F1(0, 1, bfhi, x, g0, g1, g2, g3) F1(0, 2, bflo, y, g0, g1, g2, g3)
            F1(0, 3, bfhi, y, g0, g1, g2, g3) F1(0, 4, bflo, z, g0, g1, g2, g3) F1(0, 5, bfhi, z, g0, g1, g2, g3)
            F1(1, 0, bflo, x, u0, u1, u2, u3) F1(1, 1, bfhi, x, u0, u1, u2, u3) F1(1, 2, bflo, y, u0, u1, u2, u3)
            F1(1, 3, bfhi, y, u0, u1, u2, u3) F1(1, 4, bflo, z, u0, u1, u2, u3) F1(1, 5, bfhi, z, u0, u1, u2, u3)
#undef F1
        }
        __syncthreads();
        STAMP(6);
        if (t < 576) {
            const int oi = t >> 3, sub = t & 7;
            float a = 0.f;
            for (int i = sub; i < 170; i += 8) a += f1red[oi * 170 + i];
            a += dpp<0xB1>(a); a += dpp<0x4E>(a); a += dpp<0x141>(a);
            if (sub == 0) gu[oi] = a;
        }
        __syncthreads();
        if (t < 864) {
            float h[3];
#pragma unroll
            for (int i = 0; i < 3; ++i) {
                const float ga = rbf(gu[rsl + 12 * i]), ua = rbf(gu[36 + rsl + 12 * i]);
                h[i] = rbf(rbf(ga / (1.0f + __expf(-ga))) * ua);
            }
            float* fp = &f2red[rsl * D + cpc * 8];
#define F2(c, fn) (h[0] * fn(dn0.c) + h[1] * fn(dn1.c) + h[2] * fn(dn2.c))
            *(v4f*)fp = (v4f){F2(x, bflo), F2(x, bfhi), F2(y, bflo), F2(y, bfhi)};
            *(v4f*)(fp + 4) = (v4f){F2(z, bflo), F2(z, bfhi), F2(w, bflo), F2(w, bfhi)};
#undef F2
        }
        __syncthreads();
        STAMP(7);
        if (t < 288) {
            float sx = 0.f, sy = 0.f;
#pragma unroll
            for (int i = 0; i < 12; ++i) { const v2f p = *(const v2f*)&f2red[i * D + 2 * t]; sx += p.x; sy += p.y; }
            const v4u dg = {tagA, __float_as_uint(sx), tagB, __float_as_uint(sy)};
            __builtin_amdgcn_raw_buffer_store_b128(dg, rs, (int)(WS_FG + (t >> 2) * 16384 + 8192 + (li * 4 + (t & 3)) * 16), 0, 16);
        }
        STAMP(8);
        return;
    }

    float* sc = pool;
    u32* q16 = (u32*)(pool + 512);
    float* pvred = pool + 1024;
    float* o2red = pool + 5120;
    float* og = pool + 9728;
    float* fin = pool + 10240;
    const int g = li >> 3, c = li & 7;
    const int part = t & 15, ks = t >> 4;
    const size_t kvoff = ((size_t)g * SK + (SK - WIN) + (size_t)c * 256) * DH + part * 8;
    const v4u k0 = ldg16(kc + kvoff + (size_t)ks * DH), k1 = ldg16(kc + kvoff + (size_t)(ks + 64) * DH);
    const v4u k2 = ldg16(kc + kvoff + (size_t)(ks + 128) * DH), k3 = ldg16(kc + kvoff + (size_t)(ks + 192) * DH);
    const v4u v0 = ldg16(vc + kvoff + (size_t)ks * DH), v1 = ldg16(vc + kvoff + (size_t)(ks + 64) * DH);
    const v4u v2 = ldg16(vc + kvoff + (size_t)(ks + 128) * DH), v3 = ldg16(vc + kvoff + (size_t)(ks + 192) * DH);
    const bool iso = wave < 9;
    const int cpc = t % 72, rsl = t / 72;
    const u16* op = wo + (size_t)(li * 32 + rsl) * D + cpc * 8;
    const v4u ow0 = ldg16(iso ? op : wo), ow1 = ldg16(iso ? op + (size_t)8 * D : wo), ow2 = ldg16(iso ? op + (size_t)16 * D : wo), ow3 = ldg16(iso ? op + (size_t)24 * D : wo);
    SCHED_FENCE();

    STAMP(1);
    epoch = __builtin_amdgcn_readfirstlane(epoch);
    const u32 tagA = epoch + 1u, tagB = (epoch + 1u) * 0x9E3779B1u ^ 0x7F4A7C15u;
    if (wave < 2) {
        const v4u pq = poll_until(rs, (int)(WS_QG + g * 2048 + (wave * 64 + lane) * 16), tagA, tagB, true);
        q16[wave * 64 + lane] = (pq.y >> 16) | (pq.w & 0xffff0000u);
    }
    __syncthreads();
    STAMP(2);
    {
        const v4u qa = *(const v4u*)&q16[part * 4], qb = *(const v4u*)&q16[64 + part * 4];
        float da[4], db[4];
#define SCORE(i, K) { float s0 = dot2bf(qa.x, K.x, 0.f), s1 = dot2bf(qb.x, K.x, 0.f); s0 = dot2bf(qa.y, K.y, s0); s1 = dot2bf(qb.y, K.y, s1); \
                      s0 = dot2bf(qa.z, K.z, s0); s1 = dot2bf(qb.z, K.z, s1); s0 = dot2bf(qa.w, K.w, s0); s1 = dot2bf(qb.w, K.w, s1); \
                      da[i] = row16_sum(s0); db[i] = row16_sum(s1); }
        SCORE(0, k0) SCORE(1, k1) SCORE(2, k2) SCORE(3, k3)
#undef SCORE
        if (part == 0) {
            const float scale = 0.08838834764831845f;
#pragma unroll
            for (int i = 0; i < 4; ++i) { sc[ks + 64 * i] = rbf(rbf(da[i]) * scale); sc[256 + ks + 64 * i] = rbf(rbf(db[i]) * scale); }
        }
    }
    __syncthreads();
    STAMP(3);
    float m0, m1, l0, l1, pa[4], pb[4];
    {
        const v4f a = *(const v4f*)&sc[lane * 4], bq = *(const v4f*)&sc[256 + lane * 4];
        m0 = xrow_max(row16_max(fmaxf(fmaxf(a.x, a.y), fmaxf(a.z, a.w))));
        m1 = xrow_max(row16_max(fmaxf(fmaxf(bq.x, bq.y), fmaxf(bq.z, bq.w))));
        l0 = xrow_sum(row16_sum((__expf(a.x - m0) + __expf(a.y - m0)) + (__expf(a.z - m0) + __expf(a.w - m0))));
        l1 = xrow_sum(row16_sum((__expf(bq.x - m1) + __expf(bq.y - m1)) + (__expf(bq.z - m1) + __expf(bq.w - m1))));
#pragma unroll
        for (int i = 0; i < 4; ++i) { pa[i] = __expf(sc[ks + 64 * i] - m0); pb[i] = __expf(sc[256 + ks + 64 * i] - m1); }
    }
    STAMP(4);
    {
        float o0[8], o1[8];
#define PV(i, fn, cc) { const float a = fn(v0.cc), bb = fn(v1.cc), cx = fn(v2.cc), dx = fn(v3.cc); \
                        o0[i] = (pa[0] * a + pa[1] * bb) + (pa[2] * cx + pa[3] * dx); o1[i] = (pb[0] * a + pb[1] * bb) + (pb[2] * cx + pb[3] * dx); }
        PV(0, bflo, x) PV(1, bfhi, x) PV(2, bflo, y) PV(3, bfhi, y) PV(4, bflo, z) PV(5, bfhi, z) PV(6, bflo, w) PV(7, bfhi, w)
#undef PV
#pragma unroll
        for (int i = 0; i < 8; ++i) { o0[i] = xrow_sum(o0[i]); o1[i] = xrow_sum(o1[i]); }
        if (lane < 16) {
            float* pp = &pvred[wave * 256 + part * 8];
            *(v4f*)pp = (v4f){o0[0], o0[1], o0[2], o0[3]};
            *(v4f*)(pp + 4) = (v4f){o0[4], o0[5], o0[6], o0[7]};
            *(v4f*)(pp + 128) = (v4f){o1[0], o1[1], o1[2], o1[3]};
            *(v4f*)(pp + 132) = (v4f){o1[4], o1[5], o1[6], o1[7]};
        }
    }
    __syncthreads();
    STAMP(5);
    if (t < 128) {
        float sx = 0.f, sy = 0.f;
#pragma unroll
        for (int ww = 0; ww < 16; ++ww) { const v2f pv = *(const v2f*)&pvred[ww * 256 + 2 * t]; sx += pv.x; sy += pv.y; }
        const int d = (2 * t) & 127, h = t >> 6;
        const int cons = h * 4 + (d >> 5), e = (d & 31) >> 1;
        const v4u dg = {tagA, __float_as_uint(sx), tagB, __float_as_uint(sy)};
        __builtin_amdgcn_raw_buffer_store_b128(dg, rs, (int)(WS_OG + ((g * 8 + cons) * 8 + c) * 272 + e * 16), 0, 16);
    } else if (t < 136) {
        const int cons = t - 128, h = cons >> 2;
        const v4u dg = {tagA, __float_as_uint(h ? m1 : m0), tagB, __float_as_uint(h ? l1 : l0)};
        __builtin_amdgcn_raw_buffer_store_b128(dg, rs, (int)(WS_OG + ((g * 8 + cons) * 8 + c) * 272 + 256), 0, 16);
    }
    STAMP(6);
    if (wave < 3) {
        const int oidx = wave * 64 + lane;
        const bool act = oidx < 136;
        const v4u po = poll_until(rs, (int)(WS_OG + (g * 8 + c) * 2176 + oidx * 16), tagA, tagB, act);
        if (act) { const int pc = oidx / 17, e = oidx % 17; *(v2f*)&og[pc * 36 + 2 * e] = (v2f){__uint_as_float(po.y), __uint_as_float(po.w)}; }
    }
    __syncthreads();
    STAMP(7);
    if (iso) {
        float M = og[32];
#pragma unroll
        for (int pc = 1; pc < 8; ++pc) M = fmaxf(M, og[pc * 36 + 32]);
        float L = 0.f, oa[4] = {0.f, 0.f, 0.f, 0.f};
#pragma unroll
        for (int pc = 0; pc < 8; ++pc) {
            const float e = __expf(og[pc * 36 + 32] - M);
            L += e * og[pc * 36 + 33];
#pragma unroll
            for (int i = 0; i < 4; ++i) oa[i] += e * og[pc * 36 + rsl + 8 * i];
        }
        const float rl = 1.0f / L;
#pragma unroll
        for (int i = 0; i < 4; ++i) oa[i] = rbf(oa[i] * rl);
        float* opp = &o2red[rsl * D + cpc * 8];
#define OP(cc, fn) ((oa[0] * fn(ow0.cc) + oa[1] * fn(ow1.cc)) + (oa[2] * fn(ow2.cc) + oa[3] * fn(ow3.cc)))
        *(v4f*)opp = (v4f){OP(x, bflo), OP(x, bfhi), OP(y, bflo), OP(y, bfhi)};
        *(v4f*)(opp + 4) = (v4f){OP(z, bflo), OP(z, bfhi), OP(w, bflo), OP(w, bfhi)};
#undef OP
    }
    __syncthreads();
    if (t < 288) {
        float sx = 0.f, sy = 0.f;
#pragma unroll
        for (int i = 0; i < 8; ++i) { const v2f p = *(const v2f*)&o2red[i * D + 2 * t]; sx += p.x; sy += p.y; }
        const v4u dg = {tagA, __float_as_uint(sx), tagB, __float_as_uint(sy)};
        __builtin_amdgcn_raw_buffer_store_b128(dg, rs, (int)(WS_FG + (t >> 2) * 16384 + (li * 4 + (t & 3)) * 16), 0, 16);
    }
    STAMP(8);
    if ((li & 15) >= 9) return;
    const int r = (li >> 4) * 9 + (li & 15);
    {
        const v4u pf = poll_until(rs, (int)(WS_FG + r * 16384 + t * 16), tagA, tagB, true);
        STAMP(9);
        float sx = __uint_as_float(pf.y), sy = __uint_as_float(pf.w);
        sx += dpp<0x124>(sx); sx += dpp<0x128>(sx); sy += dpp<0x124>(sy); sy += dpp<0x128>(sy);
        sx = xrow_sum(sx); sy = xrow_sum(sy);
        if (lane < 4) *(v2f*)&fin[wave * 8 + lane * 2] = (v2f){sx, sy};
    }
    __syncthreads();
    if (t < 8) {
        float s = 0.f, f = 0.f;
#pragma unroll
        for (int ww = 0; ww < 8; ++ww) { s += fin[ww * 8 + t]; f += fin[(8 + ww) * 8 + t]; }
        s = rbf(s); f = rbf(f);
        const float gt = rbf(f / (1.0f + __expf(-f)));
        const int col = 8 * r + t;
        out[col] = (u16)f2bf(bflo((u32)x[col]) + rbf(s * gt));
        if (r == 0 && t == 0) __hip_atomic_store((GAS u32*)(ws + WS_EPOCH), tagA, __ATOMIC_RELAXED, __HIP_MEMORY_SCOPE_AGENT);
    }
    STAMP(10);
}

constexpr int HQ = 32, NCH = 16, CK = WIN / NCH;
__device__ __forceinline__ void layernorm_to_lds(const u16* __restrict__ x, float* xn, float* red  ) {
    const int tid = threadIdx.x, nt = blockDim.x, lane = tid & 63, wave = tid >> 6, nw = nt >> 6;
    float s = 0.f;
    for (int i = tid; i < D; i += nt) s += bflo((u32)x[i]);
    for (int o = 32; o >= 1; o >>= 1) s += __shfl_xor(s, o);
    if (lane == 0) red[wave] = s;
    __syncthreads();
    float tot = 0.f; for (int w = 0; w < nw; ++w) tot += red[w];
    const float mean = tot * (1.0f / D);
    __syncthreads();
    float v = 0.f;
    for (int i = tid; i < D; i += nt) { float d = bflo((u32)x[i]) - mean; v += d * d; }
    for (int o = 32; o >= 1; o >>= 1) v += __shfl_xor(v, o);
    if (lane == 0) red[wave] = v;
    __syncthreads();
    float vt = 0.f; for (int w = 0; w < nw; ++w) vt += red[w];
    const float var = rbf(vt * (1.0f / D));
    const float mb = rbf(mean);
    const float den = rbf(sqrtf(rbf(var + 1e-5f)));
    for (int i = tid; i < D; i += nt) xn[i] = rbf(rbf(bflo((u32)x[i]) - mb) / den);
    __syncthreads();
}

__global__ void __launch_bounds__(256) k1(const u16* __restrict__ x, const u16* __restrict__ wq, const u16* __restrict__ wg,
                                          const u16* __restrict__ wu, float* __restrict__ q_out, float* __restrict__ h_out) {
    __shared__ float xn[D];
    __shared__ float red[8];
    __shared__ float part[4][2][32];
    layernorm_to_lds(x, xn, red);
    const int tid = threadIdx.x, lane = tid & 63, wave = tid >> 6;
    const int l4 = tid & 3, rs = tid >> 2;
    const int task = blockIdx.x;
    const bool isq = task < 128;
    const int col0 = (isq ? task : task - 128) * 32;
    const int ld = isq ? HQ * DH : FF;
    const u16* w0 = isq ? wq : wg;
    float a0[8], a1[8];
#pragma unroll
    for (int j = 0; j < 8; ++j) a0[j] = a1[j] = 0.f;
#pragma unroll
    for (int it = 0; it < 9; ++it) {
        const int row = it * 64 + rs;
        const float xv = xn[row];
        const v4u w = *(const v4u*)(w0 + (size_t)row * ld + col0 + l4 * 8);
        a0[0] += xv * bflo(w.x); a0[1] += xv * bfhi(w.x); a0[2] += xv * bflo(w.y); a0[3] += xv * bfhi(w.y);
        a0[4] += xv * bflo(w.z); a0[5] += xv * bfhi(w.z); a0[6] += xv * bflo(w.w); a0[7] += xv * bfhi(w.w);
        if (!isq) {
            const v4u u = *(const v4u*)(wu + (size_t)row * ld + col0 + l4 * 8);
            a1[0] += xv * bflo(u.x); a1[1] += xv * bfhi(u.x); a1[2] += xv * bflo(u.y); a1[3] += xv * bfhi(u.y);
            a1[4] += xv * bflo(u.z); a1[5] += xv * bfhi(u.z); a1[6] += xv * bflo(u.w); a1[7] += xv * bfhi(u.w);
        }
    }
#pragma unroll
    for (int j = 0; j < 8; ++j) {
        for (int o = 4; o <= 32; o <<= 1) { a0[j] += __shfl_xor(a0[j], o); a1[j] += __shfl_xor(a1[j], o); }
    }
    if (lane < 4) {
#pragma unroll
        for (int j = 0; j < 8; ++j) { part[wave][0][lane * 8 + j] = a0[j]; part[wave][1][lane * 8 + j] = a1[j]; }
    }
    __syncthreads();
    if (tid < 32) {
        float s0 = part[0][0][tid] + part[1][0][tid] + part[2][0][tid] + part[3][0][tid];
        float s1 = part[0][1][tid] + part[1][1][tid] + part[2][1][tid] + part[3][1][tid];
        if (isq) q_out[col0 + tid] = rbf(s0);
        else {
            const float g = rbf(s0), u = rbf(s1);
            const float sg = rbf(g / (1.0f + __expf(-g)));
            h_out[col0 + tid] = rbf(sg * u);
        }
    }
}

__global__ void __launch_bounds__(256) k2(const u16* __restrict__ kc, const u16* __restrict__ vc, const float* __restrict__ q,
                                          const float* __restrict__ h, const u16* __restrict__ wd,
                                          float* __restrict__ part_o, float* __restrict__ stats, float* __restrict__ part_f) {
    const int tid = threadIdx.x, lane = tid & 63, wave = tid >> 6;
    if (blockIdx.x >= 256) {
        const int w = blockIdx.x - 256;
        for (int j8 = tid; j8 < D / 8; j8 += 256) {
            float a[8];
#pragma unroll
            for (int j = 0; j < 8; ++j) a[j] = 0.f;
            for (int r = 0; r < 18; ++r) {
                const float hv = h[18 * w + r];
                const v4u ww = *(const v4u*)(wd + (size_t)(18 * w + r) * D + j8 * 8);
                a[0] += hv * bflo(ww.x); a[1] += hv * bfhi(ww.x); a[2] += hv * bflo(ww.y); a[3] += hv * bfhi(ww.y);
                a[4] += hv * bflo(ww.z); a[5] += hv * bfhi(ww.z); a[6] += hv * bflo(ww.w); a[7] += hv * bfhi(ww.w);
            }
#pragma unroll
            for (int j = 0; j < 8; ++j) part_f[(size_t)w * D + j8 * 8 + j] = a[j];
        }
        return;
    }
    __shared__ float qs[2][DH];
    __shared__ float sc[2][CK];
    __shared__ float red[4][2][DH];
    __shared__ float mx[2], sm[2];
    const int g = blockIdx.x >> 4, c = blockIdx.x & 15;
    if (tid < 256) qs[tid >> 7][tid & 127] = q[g * 256 + tid];
    __syncthreads();
    const int l16 = tid & 15, ks = tid >> 4;
    const size_t base = ((size_t)g * SK + (SK - WIN) + (size_t)c * CK) * DH;
    float q0[8], q1[8];
#pragma unroll
    for (int j = 0; j < 8; ++j) { q0[j] = qs[0][l16 * 8 + j]; q1[j] = qs[1][l16 * 8 + j]; }
    const float scale = 0.08838834764831845f;
    for (int it = 0; it < CK / 16; ++it) {
        const int key = it * 16 + ks;
        const v4u kk = *(const v4u*)(kc + base + (size_t)key * DH + l16 * 8);
        float d0 = q0[0] * bflo(kk.x) + q0[1] * bfhi(kk.x) + q0[2] * bflo(kk.y) + q0[3] * bfhi(kk.y) + q0[4] * bflo(kk.z) + q0[5] * bfhi(kk.z) + q0[6] * bflo(kk.w) + q0[7] * bfhi(kk.w);
        float d1 = q1[0] * bflo(kk.x) + q1[1] * bfhi(kk.x) + q1[2] * bflo(kk.y) + q1[3] * bfhi(kk.y) + q1[4] * bflo(kk.z) + q1[5] * bfhi(kk.z) + q1[6] * bflo(kk.w) + q1[7] * bfhi(kk.w);
        for (int o = 1; o <= 8; o <<= 1) { d0 += __shfl_xor(d0, o); d1 += __shfl_xor(d1, o); }
        if (l16 == 0) { sc[0][key] = rbf(rbf(d0) * scale); sc[1][key] = rbf(rbf(d1) * scale); }
    }
    __syncthreads();
    if (wave < 2) {
        float a = sc[wave][lane], b = sc[wave][lane + 64];
        float m = fmaxf(a, b);
        for (int o = 32; o >= 1; o >>= 1) m = fmaxf(m, __shfl_xor(m, o));
        a = __expf(a - m); b = __expf(b - m);
        float s = a + b;
        for (int o = 32; o >= 1; o >>= 1) s += __shfl_xor(s, o);
        sc[wave][lane] = a; sc[wave][lane + 64] = b;
        if (lane == 0) { mx[wave] = m; sm[wave] = s; }
    }
    __syncthreads();
    float o0[8], o1[8];
#pragma unroll
    for (int j = 0; j < 8; ++j) o0[j] = o1[j] = 0.f;
    for (int it = 0; it < CK / 16; ++it) {
        const int key = it * 16 + ks;
        const v4u vv = *(const v4u*)(vc + base + (size_t)key * DH + l16 * 8);
        const float p0 = sc[0][key], p1 = sc[1][key];
        o0[0] += p0 * bflo(vv.x); o0[1] += p0 * bfhi(vv.x); o0[2] += p0 * bflo(vv.y); o0[3] += p0 * bfhi(vv.y);
        o0[4] += p0 * bflo(vv.z); o0[5] += p0 * bfhi(vv.z); o0[6] += p0 * bflo(vv.w); o0[7] += p0 * bfhi(vv.w);
        o1[0] += p1 * bflo(vv.x); o1[1] += p1 * bfhi(vv.x); o1[2] += p1 * bflo(vv.y); o1[3] += p1 * bfhi(vv.y);
        o1[4] += p1 * bflo(vv.z); o1[5] += p1 * bfhi(vv.z); o1[6] += p1 * bflo(vv.w); o1[7] += p1 * bfhi(vv.w);
    }
#pragma unroll
    for (int j = 0; j < 8; ++j) {
        o0[j] += __shfl_xor(o0[j], 16); o0[j] += __shfl_xor(o0[j], 32);
        o1[j] += __shfl_xor(o1[j], 16); o1[j] += __shfl_xor(o1[j], 32);
    }
    if (lane < 16) {
#pragma unroll
        for (int j = 0; j < 8; ++j) { red[wave][0][lane * 8 + j] = o0[j]; red[wave][1][lane * 8 + j] = o1[j]; }
    }
    __syncthreads();
    {
        const int hh = tid >> 7, d = tid & 127;
        const float v = red[0][hh][d] + red[1][hh][d] + red[2][hh][d] + red[3][hh][d];
        part_o[(((size_t)g * 2 + hh) * NCH + c) * DH + d] = v;
        if (d == 0) { stats[(((size_t)g * 2 + hh) * NCH + c) * 2 + 0] = mx[hh]; stats[(((size_t)g * 2 + hh) * NCH + c) * 2 + 1] = sm[hh]; }
    }
}

__global__ void __launch_bounds__(256) k3(const float* __restrict__ part_o, const float* __restrict__ stats,
                                          const u16* __restrict__ wo, float* __restrict__ part_s) {
    __shared__ float ov[16];
    const int tid = threadIdx.x, w = blockIdx.x;
    const int hq = w >> 3, d0 = (w & 7) * 16;
    if (tid < 16) {
        float M = -INFINITY;
        for (int c = 0; c < NCH; ++c) M = fmaxf(M, stats[((size_t)hq * NCH + c) * 2]);
        float L = 0.f, acc = 0.f;
        for (int c = 0; c < NCH; ++c) {
            const float e = __expf(stats[((size_t)hq * NCH + c) * 2] - M);
            L += e * stats[((size_t)hq * NCH + c) * 2 + 1];
            acc += e * part_o[((size_t)hq * NCH + c) * DH + d0 + tid];
        }
        ov[tid] = rbf(acc / L);
    }
    __syncthreads();
    for (int j8 = tid; j8 < D / 8; j8 += 256) {
        float a[8];
#pragma unroll
        for (int j = 0; j < 8; ++j) a[j] = 0.f;
        for (int r = 0; r < 16; ++r) {
            const float hv = ov[r];
            const v4u ww = *(const v4u*)(wo + (size_t)(16 * w + r) * D + j8 * 8);
            a[0] += hv * bflo(ww.x); a[1] += hv * bfhi(ww.x); a[2] += hv * bflo(ww.y); a[3] += hv * bfhi(ww.y);
            a[4] += hv * bflo(ww.z); a[5] += hv * bfhi(ww.z); a[6] += hv * bflo(ww.w); a[7] += hv * bfhi(ww.w);
        }
#pragma unroll
        for (int j = 0; j < 8; ++j) part_s[(size_t)w * D + j8 * 8 + j] = a[j];
    }
}

__global__ void __launch_bounds__(64) k4(const float* __restrict__ part_s, const float* __restrict__ part_f,
                                         const u16* __restrict__ x, u16* __restrict__ out) {
    const int j = blockIdx.x * 64 + threadIdx.x;
    float s = 0.f, f = 0.f;
    for (int w = 0; w < 256; ++w) { s += part_s[(size_t)w * D + j]; f += part_f[(size_t)w * D + j]; }
    s = rbf(s); f = rbf(f);
    const float g = rbf(f / (1.0f + __expf(-f)));
    out[j] = f2bf(bflo((u32)x[j]) + rbf(s * g));
}

extern "C" void kernel_launch(void* const* d_in, const int* in_sizes, int n_in,
                              void* d_out, int out_size, void* d_ws, size_t ws_size,
                              hipStream_t stream) {
    int dev = 0, cus = 0;
    (void)hipGetDevice(&dev);
    (void)hipDeviceGetAttribute(&cus, hipDeviceAttributeMultiprocessorCount, dev);
    if (cus < NWG) {
        const u16* x = (const u16*)d_in[0];
        float* wsf = (float*)d_ws;
        float* q = wsf; float* h = q + 4096; float* part_o = h + 4608; float* stats = part_o + 32 * 16 * 128;
        float* part_f = stats + 1024; float* part_s = part_f + 256 * 576;
        k1<<<272, 256, 0, stream>>>(x, (const u16*)d_in[3], (const u16*)d_in[6], (const u16*)d_in[7], q, h);
        k2<<<512, 256, 0, stream>>>((const u16*)d_in[1], (const u16*)d_in[2], q, h, (const u16*)d_in[8], part_o, stats, part_f);
        k3<<<256, 256, 0, stream>>>(part_o, stats, (const u16*)d_in[4], part_s);
        k4<<<9, 64, 0, stream>>>(part_s, part_f, x, (u16*)d_out);
        return;
    }
    fused_k<<<NWG, NT, 0, stream>>>((const u16*)d_in[0], (const u16*)d_in[1], (const u16*)d_in[2], (const u16*)d_in[3], (const u16*)d_in[4],
                                    (const u16*)d_in[6], (const u16*)d_in[7], (const u16*)d_in[8], (u16*)d_out, (unsigned char*)d_ws);
}
```
